```python
import torch

def _softmax(x, axis=-1):
    return torch.softmax(x, dim=axis)
def _gelu(x):
    return torch.nn.functional.gelu(x, approximate="tanh")
def _silu(x):
    return torch.nn.functional.silu(x)
def _sigmoid(x):
    return torch.sigmoid(x)
def _layernorm(x, axis=-1, eps=1e-5):
    m = x.mean(dim=axis, keepdim=True)
    v = x.var(dim=axis, keepdim=True, unbiased=False)
    return (x - m) / torch.sqrt(v + eps)
def _scatter_add(tgt, idx, src, axis=0):
    return tgt.clone().index_add_(axis, idx, src)
def _segment_sum(src, seg, n):
    out = torch.zeros((n,) + src.shape[1:], dtype=src.dtype)
    return out.index_add_(0, seg, src)
def _pad(x, pw):
    flat = tuple(v for pair in reversed(pw) for v in pair)
    return torch.nn.functional.pad(x, flat)

kernel_name = 'sequential_preLN__softmax_full_mha__geglu_ffn__prompt'

def setup_inputs():

    _w = (lambda a: (lambda t: t.to(torch.bfloat16) if t.is_floating_point() else t)(torch.as_tensor(a)))
    def _A_setup():
        B, S, D = 1, 2048, 640
        return {
            "x": _w((lambda *s: torch.randn(*s, dtype=torch.bfloat16) * 0.05)(B, S, D)),
            "wqkv": _w((lambda *s: torch.randn(*s, dtype=torch.bfloat16) * 0.05)(D, 3 * D)),
            "wo": _w((lambda *s: torch.randn(*s, dtype=torch.bfloat16) * 0.05)(D, D)),
        }
    _A_in = _A_setup()

    def _B_setup():
        B, S, D, F = 1, 2048, 640, 1536
        return {
            "x": _w((lambda *s: torch.randn(*s, dtype=torch.bfloat16) * 0.05)(B, S, D)),
            "w_gate": _w((lambda *s: torch.randn(*s, dtype=torch.bfloat16) * 0.05)(D, F)),
            "w_up": _w((lambda *s: torch.randn(*s, dtype=torch.bfloat16) * 0.05)(D, F)),
            "w_down": _w((lambda *s: torch.randn(*s, dtype=torch.bfloat16) * 0.05)(F, D)),
        }
    _B_in = _B_setup()

    return {
        "A_x": _A_in["x"],
        "A_wqkv": _A_in["wqkv"],
        "A_wo": _A_in["wo"],
        "B_x": _B_in["x"],
        "B_w_gate": _B_in["w_gate"],
        "B_w_up": _B_in["w_up"],
        "B_w_down": _B_in["w_down"],
    }

def reference(A_x, A_wqkv, A_wo, B_x, B_w_gate, B_w_up, B_w_down):
    def _A_ref(x, wqkv, wo):
        B, S, D = x.shape
        H = 32; Dh = D // H
        qkv = (lambda x, *d: x.permute(*d))((x @ wqkv).reshape(B, S, 3, H, Dh), 2, 0, 3, 1, 4)
        q, k, v = qkv[0], qkv[1], qkv[2]
        scale = Dh ** -0.5
        scores = torch.einsum("bhqd,bhkd->bhqk", q, k) * scale
        p = _softmax(scores, axis=-1)
        o = torch.einsum("bhqk,bhkd->bhqd", p, v)
        return (lambda x, *d: x.permute(*d))(o, 0, 2, 1, 3).reshape(B, S, D) @ wo

    def _B_ref(x, w_gate, w_up, w_down):
        return (_gelu(x @ w_gate) * (x @ w_up)) @ w_down

    x = A_x
    h = x + _A_ref(_layernorm(x, axis=-1), A_wqkv, A_wo)
    h2 = h + _B_ref(_layernorm(h, axis=-1), B_w_gate, B_w_up, B_w_down)
    return h2
```

```cpp
#include <hip/hip_runtime.h>
#include <stdint.h>

#define DI __device__ __forceinline__
typedef unsigned short u16;
typedef float f32x2 __attribute__((ext_vector_type(2)));
typedef float f32x4 __attribute__((ext_vector_type(4)));
typedef float f32x8 __attribute__((ext_vector_type(8)));
typedef float f32x16 __attribute__((ext_vector_type(16)));
typedef float f32x32 __attribute__((ext_vector_type(32)));
typedef __bf16 bf16x2 __attribute__((ext_vector_type(2)));
typedef __bf16 bf16x8 __attribute__((ext_vector_type(8)));
typedef __bf16 bf16x16 __attribute__((ext_vector_type(16)));
typedef short s16x4 __attribute__((ext_vector_type(4)));
typedef short s16x8 __attribute__((ext_vector_type(8)));
typedef unsigned u32x2 __attribute__((ext_vector_type(2)));
typedef unsigned u32x4 __attribute__((ext_vector_type(4)));
typedef __attribute__((address_space(3))) char* lds_t;

constexpr size_t OFF_Q = 0;
constexpr size_t OFF_KV = 3u << 20;
constexpr size_t OFF_AT = 9u << 20;
constexpr size_t OFF_H = 12u << 20;
constexpr size_t OFF_ST = 15u << 20;
constexpr size_t OFF_ACT = 16u << 20;
constexpr int PKT = 5248, KVH = 32 * PKT, QH = 64 * 1280;

#define SB() __builtin_amdgcn_sched_barrier(0)

DI void glds(unsigned ldsaddr, const void* sbase, unsigned voff) {
  asm volatile("s_mov_b32 m0, %0\n\ts_nop 0\n\tglobal_load_lds_dwordx4 %1, %2" ::"s"(ldsaddr), "v"(voff), "s"(sbase) : "memory");
}
DI void gldsv(unsigned ldsaddr, const void* vaddr) {
  asm volatile("s_mov_b32 m0, %0\n\ts_nop 0\n\tglobal_load_lds_dwordx4 %1, off" ::"s"(ldsaddr), "v"(vaddr) : "memory");
}
template <int IMM> DI void gld4(u32x4& d, const void* sbase, unsigned voff) {
  asm volatile("global_load_dwordx4 %0, %1, %2 offset:%c3" : "=v"(d) : "v"(voff), "s"(sbase), "i"(IMM) : "memory");
}
template <int IMM> DI void gld2(u32x2& d, const void* sbase, unsigned voff) {
  asm volatile("global_load_dwordx2 %0, %1, %2 offset:%c3" : "=v"(d) : "v"(voff), "s"(sbase), "i"(IMM) : "memory");
}
template <int N> DI void wait_vm() { asm volatile("s_waitcnt vmcnt(%c0)" ::"i"(N) : "memory"); }
template <int N> DI void wait_vm4(u32x4& a, u32x4& b, u32x4& c, u32x4& d) {
  asm volatile("s_waitcnt vmcnt(%c4)" : "+v"(a), "+v"(b), "+v"(c), "+v"(d) : "i"(N) : "memory");
}
DI void bar() { asm volatile("s_barrier" ::: "memory"); }
DI void bar_lgkm() { asm volatile("s_waitcnt lgkmcnt(0)\n\ts_barrier" ::: "memory"); }
DI u32x4 lds16(lds_t p) { return *(__attribute__((address_space(3))) u32x4*)p; }
DI u32x2 lds8(lds_t p) { return *(__attribute__((address_space(3))) u32x2*)p; }
DI f32x4 ldsf4(lds_t p) { return *(__attribute__((address_space(3))) f32x4*)p; }
DI float ldsf(lds_t p) { return *(__attribute__((address_space(3))) float*)p; }
DI void stsf(lds_t p, float v) { *(__attribute__((address_space(3))) float*)p = v; }
DI void stsf4(lds_t p, f32x4 v) { *(__attribute__((address_space(3))) f32x4*)p = v; }
DI s16x4 trd(lds_t p) { return __builtin_amdgcn_ds_read_tr16_b64_v4i16((__attribute__((address_space(3))) s16x4*)p); }
DI bf16x8 trd2(lds_t p0, lds_t p1) {
  s16x4 a = trd(p0), b = trd(p1);
  return __builtin_bit_cast(bf16x8, __builtin_shufflevector(a, b, 0, 1, 2, 3, 4, 5, 6, 7));
}
DI bf16x8 B8(u32x4 v) { return __builtin_bit_cast(bf16x8, v); }
DI unsigned pk(float a, float b) {
  f32x2 t = {a, b};
  return __builtin_bit_cast(unsigned, __builtin_convertvector(t, bf16x2));
}
DI float blo(unsigned v) { return __builtin_bit_cast(float, v << 16); }
DI float bhi(unsigned v) { return __builtin_bit_cast(float, v & 0xffff0000u); }
DI f32x16 mm32(bf16x8 a, bf16x8 b, f32x16 c) { return __builtin_amdgcn_mfma_f32_32x32x16_bf16(a, b, c, 0, 0, 0); }
DI f32x4 mm16(bf16x8 a, bf16x8 b, f32x4 c) { return __builtin_amdgcn_mfma_f32_16x16x32_bf16(a, b, c, 0, 0, 0); }
DI bf16x8 selA(int l) {
  unsigned v = ((l & 15) == ((l >> 4) & 1)) ? 0x3f803f80u : 0u;
  u32x4 t = {v, v, v, v};
  return B8(t);
}

extern __shared__ __attribute__((aligned(16))) char smem_g[];

struct FrA { bf16x8 x0, x1, w0, w1; };
template <bool VT> DI void a_consume(lds_t sm, int wm, int wn, int l, f32x16 (&acc)[2][2], f32x4& cs) {
  const int h = l >> 5, r = l & 31, swz = (r >> 1) & 7;
  unsigned xa[4];
#pragma unroll
  for (int kk = 0; kk < 4; kk++) xa[kk] = (64 * wm + r) * 128 + (((2 * kk + h) ^ swz) << 4);
  const unsigned wa = 16384 + (h * 4 + 2 * wn) * 512 + ((l & 15) >> 2) * 64 + ((l >> 4) & 1) * 32 + (l & 3) * 8;
  const bf16x8 sel = selA(l);
  auto rd = [&](unsigned so, int kk) {
    FrA f;
    lds_t px = sm + (xa[kk] + so), pw = sm + (wa + so) + kk * 4096;
    f.x0 = B8(lds16(px));
    f.w0 = trd2(pw, pw + 256);
    f.x1 = B8(lds16(px + 4096));
    f.w1 = trd2(pw + 512, pw + 768);
    return f;
  };
  auto m1 = [&](const FrA& f) { acc[0][0] = VT ? mm32(f.x0, f.w0, acc[0][0]) : mm32(f.w0, f.x0, acc[0][0]); };
  auto m2 = [&](const FrA& f) {
    acc[1][0] = VT ? mm32(f.x1, f.w0, acc[1][0]) : mm32(f.w0, f.x1, acc[1][0]);
    acc[0][1] = VT ? mm32(f.x0, f.w1, acc[0][1]) : mm32(f.w1, f.x0, acc[0][1]);
    acc[1][1] = VT ? mm32(f.x1, f.w1, acc[1][1]) : mm32(f.w1, f.x1, acc[1][1]);
    cs = mm16(sel, wm ? f.w1 : f.w0, cs);
  };
  bar();
  FrA cur = rd(0, 0);
#pragma unroll 1
  for (int s = 0; s < 10; s++) {
    const unsigned so = (s & 3) << 15, sn = ((s + 1) & 3) << 15;
    FrA n1 = rd(so, 1); SB(); m1(cur); m2(cur); SB();
    FrA n2 = rd(so, 2); SB(); m1(n1); m2(n1); SB();
    FrA n3 = rd(so, 3); SB(); m1(n2); m2(n2); SB();
    m1(n3); SB();
    bar_lgkm();
    cur = rd(sn, 0); SB();
    m2(n3); SB();
  }
}

__global__ __launch_bounds__(512) void k_qkv(const u16* __restrict__ x, const u16* __restrict__ w, char* __restrict__ ws) {
  lds_t sm = (lds_t)smem_g;
  const int b = blockIdx.x, g = b & 7, j = b >> 3;
  const int mt = 4 * (g >> 1) + (j & 3), nt = 8 * (g & 1) + (j >> 2);
  if (nt >= 15) return;
  const int wv = __builtin_amdgcn_readfirstlane(threadIdx.x >> 6), l = threadIdx.x & 63;
  constexpr int SA = 131072, SBB = SA + 512, SC = SA + 1024;
  if (wv >= 4) {
    const int hw = wv - 4;
    unsigned xo[4], wo[4];
#pragma unroll
    for (int i = 0; i < 4; i++) {
      int row = 32 * hw + 8 * i + (l >> 3);
      xo[i] = row * 1280 + (((l & 7) ^ ((row >> 1) & 7)) << 4);
      int k8 = 2 * hw + (i >> 1), u = i & 1;
      wo[i] = (8 * k8 + ((l >> 2) & 7)) * 3840 + u * 128 + (l >> 5) * 64 + (l & 3) * 16;
    }
    const char* xb = (const char*)x + (size_t)mt * 128 * 1280;
    const char* wb = (const char*)w + nt * 256;
    auto issue = [&](int s) {
      unsigned so = (unsigned)(uintptr_t)sm + ((s & 3) << 15) + hw * 4096;
      const char* xs = xb + s * 128;
      const char* wsrc = wb + (size_t)s * 64 * 3840;
#pragma unroll
      for (int i = 0; i < 4; i++) glds(so + i * 1024, xs, xo[i]);
#pragma unroll
      for (int i = 0; i < 4; i++) glds(so + 16384 + i * 1024, wsrc, wo[i]);
    };
    issue(0); issue(1); issue(2); issue(3);
    wait_vm<24>(); bar();
    float sum[4] = {0, 0, 0, 0}, sq[4] = {0, 0, 0, 0};
#pragma unroll 1
    for (int s = 0; s < 10; s++) {
      lds_t p = sm + (((s & 3) << 15) + hw * 4096 + l * 16);
#pragma unroll
      for (int i = 0; i < 4; i++) {
        u32x4 v = lds16(p + i * 1024);
#pragma unroll
        for (int e = 0; e < 4; e++) {
          float a = blo(v[e]), c = bhi(v[e]);
          sum[i] += a; sq[i] = __builtin_fmaf(a, a, sq[i]);
          sum[i] += c; sq[i] = __builtin_fmaf(c, c, sq[i]);
        }
      }
      if (s < 7) wait_vm<16>(); else if (s == 7) wait_vm<8>(); else wait_vm<0>();
      bar_lgkm();
      if (s < 6) issue(s + 4);
    }
#pragma unroll
    for (int i = 0; i < 4; i++) {
      float a = sum[i], q = sq[i];
      a += __shfl_xor(a, 1); q += __shfl_xor(q, 1);
      a += __shfl_xor(a, 2); q += __shfl_xor(q, 2);
      a += __shfl_xor(a, 4); q += __shfl_xor(q, 4);
      float mean = a * (1.f / 640.f);
      float var = fmaxf(q * (1.f / 640.f) - mean * mean, 0.f);
      float rs = 1.f / sqrtf(var + 1e-5f);
      if ((l & 7) == 0) {
        int row = 32 * hw + 8 * i + (l >> 3);
        stsf(sm + SA + row * 4, rs);
        stsf(sm + SBB + row * 4, -mean * rs);
      }
    }
    bar_lgkm();
    return;
  }
  const int wm = wv & 1, wn = wv >> 1, h = l >> 5, r = l & 31;
  f32x16 acc[2][2] = {};
  f32x4 cs = {};
  const bool vt = nt >= 10;
  if (vt) a_consume<true>(sm, wm, wn, l, acc, cs); else a_consume<false>(sm, wm, wn, l, acc, cs);
  if (l < 16) {
    stsf(sm + SC + (64 * wn + 32 * wm + l) * 4, cs[0]);
    stsf(sm + SC + (64 * wn + 32 * wm + 16 + l) * 4, cs[1]);
  }
  bar_lgkm();
  if (!vt) {
    const bool isq = nt < 5;
    const float sc = isq ? 0.22360679774997896f * 1.4426950408889634f : 1.f;
    const int hs = isq ? QH : KVH;
    char* base = ws + (isq ? OFF_Q : OFF_KV);
    const int c0 = 128 * nt - (isq ? 0 : 640) + 64 * wn + 4 * h;
#pragma unroll
    for (int mi = 0; mi < 2; mi++) {
      const int tl = 64 * wm + 32 * mi + r;
      const float a = ldsf(sm + SA + tl * 4) * sc, bb = ldsf(sm + SBB + tl * 4) * sc;
      const int T = 4 * mt + 2 * wm + mi;
      int om, orr;
      if (isq) { om = T * 1280 + r * 16; orr = T * 1280 + 1024 + r * 8; }
      else {
        int rho = (r & 19) | ((r & 4) << 1) | ((r & 8) >> 1);
        int p = ((T >> 1) - 1) & 31;
        om = p * PKT + mi * 1024 + rho * 16; orr = p * PKT + 2048 + (mi * 32 + rho) * 8;
      }
#pragma unroll
      for (int ni = 0; ni < 2; ni++) {
#pragma unroll
        for (int i = 0; i < 4; i++) {
          f32x4 c4 = ldsf4(sm + SC + (64 * wn + 32 * ni + 8 * i + 4 * h) * 4);
          int c = c0 + 32 * ni + 8 * i;
          int head = (c * 3277) >> 16, d = c - 20 * head;
          int off = head * hs + (d < 16 ? om + (d >> 3) * 512 + (d & 4) * 2 : orr);
          const f32x16& A = acc[mi][ni];
          u32x2 o;
          o[0] = pk(a * A[4 * i] + bb * c4[0], a * A[4 * i + 1] + bb * c4[1]);
          o[1] = pk(a * A[4 * i + 2] + bb * c4[2], a * A[4 * i + 3] + bb * c4[3]);
          *(u32x2*)(base + off) = o;
        }
      }
    }
  } else {
    char* base = ws + OFF_KV + (2 * mt + wm) * PKT + 2560 + h * 336;
#pragma unroll
    for (int ni = 0; ni < 2; ni++) {
      const int c = 128 * nt - 1280 + 64 * wn + 32 * ni + r;
      const int head = (c * 3277) >> 16, d = c - 20 * head;
      const float cv = ldsf(sm + SC + (64 * wn + 32 * ni + r) * 4);
      char* pb = base + head * KVH + d * 16;
#pragma unroll
      for (int mi = 0; mi < 2; mi++) {
        const f32x16& A = acc[mi][ni];
#pragma unroll
        for (int s = 0; s < 2; s++) {
          u32x4 o;
#pragma unroll
          for (int u = 0; u < 2; u++) {
            int i = 2 * s + u, tl = 64 * wm + 32 * mi + 8 * i + 4 * h;
            f32x4 a4 = ldsf4(sm + SA + tl * 4), b4 = ldsf4(sm + SBB + tl * 4);
            o[2 * u] = pk(a4[0] * A[4 * i] + b4[0] * cv, a4[1] * A[4 * i + 1] + b4[1] * cv);
            o[2 * u + 1] = pk(a4[2] * A[4 * i + 2] + b4[2] * cv, a4[3] * A[4 * i + 3] + b4[3] * cv);
          }
          *(u32x4*)(pb + 672 * (2 * mi + s)) = o;
          if (d == 0) {
            u32x4 one = {0x3f803f80u, 0x3f803f80u, 0x3f803f80u, 0x3f803f80u};
            *(u32x4*)(pb + 672 * (2 * mi + s) + 320) = one;
          }
        }
      }
    }
  }
}

DI f32x32 cat16(f32x16 a, f32x16 b) {
  return __builtin_shufflevector(a, b, 0, 1, 2, 3, 4, 5, 6, 7, 8, 9, 10, 11, 12, 13, 14, 15, 16, 17, 18, 19, 20, 21, 22, 23, 24, 25, 26, 27, 28, 29, 30, 31);
}
template <int O> DI bf16x16 sub8(const f32x32& s) {
  f32x8 t = __builtin_shufflevector(s, s, O, O + 1, O + 2, O + 3, O + 4, O + 5, O + 6, O + 7);
  return __builtin_bit_cast(bf16x16, t);
}
DI f32x16 smf(bf16x8 a, bf16x16 b, f32x16 c) { return __builtin_amdgcn_smfmac_f32_32x32x32_bf16(a, b, c, 0xDDDD, 0, 0); }
DI f32x32 res2(u32x2 a, u32x2 b, f32x32 c) {
  return __builtin_amdgcn_mfma_f32_32x32x4bf16_1k(__builtin_bit_cast(s16x4, a), __builtin_bit_cast(s16x4, b), c, 0, 0, 0);
}
template <int O> DI void exp8(f32x32& s) {
#pragma unroll
  for (int i = O; i < O + 8; i++) s[i] = __builtin_amdgcn_exp2f(s[i]);
}
DI float xor32(float v) { return __shfl_xor(v, 32); }

constexpr int SLOT = 21504;
DI void attn_step(lds_t kp, lds_t rp, lds_t vp, bf16x8 q, u32x2 qr, const f32x16& negm, f32x32& P, f32x32& N, f32x16& O) {
  bf16x8 k0 = B8(lds16(kp)), k1 = B8(lds16(kp + 1024));
  u32x2 kr = lds8(rp);
  bf16x8 v0 = B8(lds16(vp)), v1 = B8(lds16(vp + 672)), v2 = B8(lds16(vp + 1344)), v3 = B8(lds16(vp + 2016));
  SB();
  exp8<0>(P); SB();
  f32x16 lo = mm32(k0, q, negm); SB();
  exp8<8>(P); SB();
  O = smf(v0, sub8<0>(P), O);
  f32x16 hi = mm32(k1, q, negm); SB();
  exp8<16>(P); SB();
  O = smf(v1, sub8<8>(P), O);
  N = res2(kr, qr, cat16(lo, hi)); SB();
  exp8<24>(P); SB();
  O = smf(v2, sub8<16>(P), O);
  O = smf(v3, sub8<24>(P), O); SB();
}

__global__ __launch_bounds__(512) void k_attn(char* __restrict__ ws) {
  lds_t sm = (lds_t)smem_g;
  const int b = blockIdx.x, g = b & 7, j = b >> 3;
  const int head = 4 * g + (j >> 3), qb = j & 7;
  const int wv = __builtin_amdgcn_readfirstlane(threadIdx.x >> 6), l = threadIdx.x & 63, h = l >> 5, r = l & 31;
  const char* kv = ws + OFF_KV + (size_t)head * KVH;
  const int T = 8 * qb + wv;
  const char* qp = ws + OFF_Q + (size_t)head * QH + T * 1280;
  u32x4 qv, k0v, k1v; u32x2 qr, krv;
  gld4<0>(qv, qp, l * 16);
  gld2<1024>(qr, qp, r * 8);
  gld4<0>(k0v, kv + 31 * PKT, l * 16);
  gld4<1024>(k1v, kv + 31 * PKT, l * 16);
  gld2<2048>(krv, kv + 31 * PKT, l * 8);
  auto issue = [&](int c) {
    unsigned so = (unsigned)(uintptr_t)sm + (c & 1) * SLOT + wv * 1024;
    const char* src = kv + c * (4 * PKT) + wv * 1024;
    glds(so, src, l * 16);
    glds(so + 8192, src + 8192, l * 16);
    if (wv < 5) glds(so + 16384, src + 16384, l * 16);
  };
  issue(0);
  asm volatile("s_waitcnt vmcnt(2)" : "+v"(qv), "+v"(qr), "+v"(k0v), "+v"(k1v), "+v"(krv)::"memory");
  const bf16x8 q = B8(qv);
  f32x32 P, N;
  f32x16 negm, O = {};
  {
    f32x16 z = {};
    P = res2(krv, qr, cat16(mm32(B8(k0v), q, z), mm32(B8(k1v), q, z)));
    float m = P[0];
#pragma unroll
    for (int i = 1; i < 32; i++) m = fmaxf(m, P[i]);
    m = fmaxf(m, xor32(m));
#pragma unroll
    for (int i = 0; i < 32; i++) P[i] -= m;
#pragma unroll
    for (int i = 0; i < 16; i++) negm[i] = -m;
  }
  const int vrow = r <= 20 ? r : (r < 28 ? 20 : 4);
  const unsigned ka = l * 16, ra = 2048 + l * 8, va = 2560 + h * 336 + vrow * 16;
#pragma unroll 1
  for (int c = 0; c < 8; c++) {
    asm volatile("s_waitcnt vmcnt(0) lgkmcnt(0)\n\ts_barrier" ::: "memory");
    if (c < 7) issue(c + 1);
    const unsigned so = (c & 1) * SLOT;
    lds_t kp = sm + (ka + so), rp = sm + (ra + so), vp = sm + (va + so);
    attn_step(kp, rp, vp, q, qr, negm, P, N, O);
    attn_step(kp + PKT, rp + PKT, vp + PKT, q, qr, negm, N, P, O);
    attn_step(kp + 2 * PKT, rp + 2 * PKT, vp + 2 * PKT, q, qr, negm, P, N, O);
    attn_step(kp + 3 * PKT, rp + 3 * PKT, vp + 3 * PKT, q, qr, negm, N, P, O);
  }
  float ls = h ? O[8] : O[12];
  float mag = 0.f;
#pragma unroll
  for (int i = 0; i < 8; i++) mag = fmaxf(mag, fabsf(O[i]));
  if (!h) {
#pragma unroll
    for (int i = 8; i < 12; i++) mag = fmaxf(mag, fabsf(O[i]));
  }
  bool bad = !(ls < 1e30f) || !(mag < 3e38f);
  if (__builtin_amdgcn_ballot_w64(bad) != 0) {
    float m = -INFINITY;
    O = f32x16{};
#pragma unroll 1
    for (int i = 0; i < 32; i++) {
      const char* pk_ = kv + ((i + 31) & 31) * PKT;
      const char* pv_ = kv + i * PKT + va;
      f32x16 z = {};
      f32x32 S = res2(*(const u32x2*)(pk_ + ra), qr, cat16(mm32(B8(*(const u32x4*)(pk_ + ka)), q, z), mm32(B8(*(const u32x4*)(pk_ + ka + 1024)), q, z)));
      float mn = m;
#pragma unroll
      for (int t = 0; t < 32; t++) mn = fmaxf(mn, S[t]);
      mn = fmaxf(mn, xor32(mn));
      float al = __builtin_amdgcn_exp2f(m - mn);
      m = mn;
#pragma unroll
      for (int t = 0; t < 32; t++) S[t] = __builtin_amdgcn_exp2f(S[t] - mn);
#pragma unroll
      for (int t = 0; t < 16; t++) O[t] *= al;
      O = smf(B8(*(const u32x4*)(pv_)), sub8<0>(S), O);
      O = smf(B8(*(const u32x4*)(pv_ + 672)), sub8<8>(S), O);
      O = smf(B8(*(const u32x4*)(pv_ + 1344)), sub8<16>(S), O);
      O = smf(B8(*(const u32x4*)(pv_ + 2016)), sub8<24>(S), O);
    }
    ls = h ? O[8] : O[12];
  }
  const float inv = 1.f / ls;
  const int tok = 32 * T + r;
  char* ob = ws + OFF_AT + (size_t)(tok >> 4) * 20 * 1024 + (tok & 15) * 16;
#pragma unroll
  for (int i = 0; i < 3; i++) {
    int k = head * 20 + 8 * i + 4 * h;
    u32x2 o;
    o[0] = pk(O[4 * i] * inv, O[4 * i + 1] * inv);
    o[1] = pk(O[4 * i + 2] * inv, O[4 * i + 3] * inv);
    if (i < 2 || !h) *(u32x2*)(ob + (k >> 5) * 1024 + ((k >> 3) & 3) * 256 + (k & 4) * 2) = o;
  }
}

template <int NKB> DI void ce_mma(lds_t wp, const u32x4* a, f32x4 (&acc)[5], lds_t wp2) {
#pragma unroll
  for (int kb = 0; kb < NKB; kb++) {
#pragma unroll
    for (int t = 0; t < 5; t++) {
      bf16x8 wf = trd2(wp + kb * 5120 + t * 32, wp2 + kb * 5120 + t * 32);
      acc[t] = mm16(wf, B8(a[kb]), acc[t]);
    }
  }
}
template <bool ISE> __global__ __launch_bounds__(512) void k_ce(const u16* __restrict__ w, const u16* __restrict__ x, char* __restrict__ ws, u16* __restrict__ out) {
  lds_t sm = (lds_t)smem_g;
  constexpr int KBH = ISE ? 24 : 10;
  const int b = blockIdx.x, g = b & 7, j = b >> 3;
  const int row0 = 256 * g + 64 * (j >> 3), n0 = 80 * (j & 7);
  const int wv = __builtin_amdgcn_readfirstlane(threadIdx.x >> 6), l = threadIdx.x & 63;
  const int wm = wv & 3, kh = wv >> 2, G = l >> 4, tk = l & 15;
  unsigned wo[5];
#pragma unroll
  for (int i = 0; i < 5; i++) {
    int ci = 64 * i + l, rr = (ci * 6554) >> 16, c = ci - 10 * rr;
    int src = (rr & 8) ? (rr ^ 4) : rr;
    wo[i] = src * 1280 + c * 16;
  }
  const char* wb = (const char*)w + n0 * 2 + (size_t)(kh * KBH + wm) * 32 * 1280;
  auto issue = [&](int s) {
    unsigned so = (unsigned)(uintptr_t)sm + (s & 3) * 40960 + wv * 5120;
    const char* src = wb + (size_t)s * 4 * 32 * 1280;
#pragma unroll
    for (int i = 0; i < 5; i++) glds(so + i * 1024, src, wo[i]);
  };
  const char* ab = ws + (ISE ? OFF_ACT : OFF_AT) + (size_t)((row0 >> 4) + wm) * (2 * KBH) * 1024 + kh * KBH * 1024;
  u32x4 a[KBH];
  const unsigned al = l * 16;
#define LDA(s) { gld4<0>(a[4 * (s)], ab + (s) * 4096, al); gld4<1024>(a[4 * (s) + 1], ab + (s) * 4096, al); \
    if (4 * (s) + 2 < KBH) { gld4<2048>(a[(4 * (s) + 2) % KBH], ab + (s) * 4096, al); gld4<3072>(a[(4 * (s) + 3) % KBH], ab + (s) * 4096, al); } }
  const int tok = row0 + 16 * wm + tk;
  u32x2 rs[5];
  const unsigned hoff = ((tok >> 5) * 40 + (n0 >> 4)) * 1024 + ((G >> 1) * 32 + (tok & 31)) * 16 + (G & 1) * 8;
  const unsigned wr = kh * 20480 + (8 * G + (tk >> 2)) * 160 + (l & 3) * 8;
  const unsigned w0 = wr + (G & 1) * 640, w1 = wr + ((G & 1) ^ 1) * 640;
  f32x4 acc[5] = {};
  if constexpr (ISE) {
    issue(0); LDA(0) issue(1); LDA(1) issue(2); LDA(2) issue(3); LDA(3) LDA(4) LDA(5)
    gld2<0>(rs[0], ws + OFF_H, hoff); gld2<1024>(rs[1], ws + OFF_H, hoff); gld2<2048>(rs[2], ws + OFF_H, hoff);
    gld2<3072>(rs[3], ws + OFF_H, hoff); gld2<0>(rs[4], ws + OFF_H + 4096, hoff);
    wait_vm4<40>(a[0], a[1], a[2], a[3]); bar();
    ce_mma<4>(sm + w0, a, acc, sm + w1);
    wait_vm4<31>(a[4], a[5], a[6], a[7]); bar_lgkm(); issue(4);
    ce_mma<4>(sm + w0 + 40960, a + 4, acc, sm + w1 + 40960);
    wait_vm4<27>(a[8], a[9], a[10], a[11]); bar_lgkm(); issue(5);
    ce_mma<4>(sm + w0 + 81920, a + 8, acc, sm + w1 + 81920);
    wait_vm4<23>(a[12], a[13], a[14], a[15]); bar();
    ce_mma<4>(sm + w0 + 122880, a + 12, acc, sm + w1 + 122880);
    wait_vm4<5>(a[16], a[17], a[18], a[19]); bar();
    ce_mma<4>(sm + w0, a + 16, acc, sm + w1);
    wait_vm4<0>(a[20], a[21], a[22], a[23]); bar();
    ce_mma<4>(sm + w0 + 40960, a + 20, acc, sm + w1 + 40960);
  } else {
    const unsigned xoff = tok * 1280 + (n0 + 4 * G) * 2;
    issue(0); LDA(0) issue(1); LDA(1)
    if (wm < 2) issue(2);
    LDA(2)
    gld2<0>(rs[0], x, xoff); gld2<32>(rs[1], x, xoff); gld2<64>(rs[2], x, xoff); gld2<96>(rs[3], x, xoff); gld2<128>(rs[4], x, xoff);
    wait_vm4<16>(a[0], a[1], a[2], a[3]); bar();
    ce_mma<4>(sm + w0, a, acc, sm + w1);
    wait_vm4<7>(a[4], a[5], a[6], a[7]); bar();
    ce_mma<4>(sm + w0 + 40960, a + 4, acc, sm + w1 + 40960);
    asm volatile("s_waitcnt vmcnt(0)" : "+v"(a[8]), "+v"(a[9])::"memory"); bar();
    ce_mma<2>(sm + w0 + 81920, a + 8, acc, sm + w1 + 81920);
  }
  asm volatile("" : "+v"(rs[0]), "+v"(rs[1]), "+v"(rs[2]), "+v"(rs[3]), "+v"(rs[4]));
  lds_t rb = sm + (122880 + wm * 5120 + l * 16);
  if (kh) {
#pragma unroll
    for (int t = 0; t < 5; t++) stsf4(rb + t * 1024, acc[t]);
  }
  bar_lgkm();
  if (kh) return;
  float sum = 0.f, sq = 0.f;
#pragma unroll
  for (int t = 0; t < 5; t++) {
    f32x4 o = acc[t] + ldsf4(rb + t * 1024);
    u32x2 y;
    y[0] = pk(o[0] + blo(rs[t][0]), o[1] + bhi(rs[t][0]));
    y[1] = pk(o[2] + blo(rs[t][1]), o[3] + bhi(rs[t][1]));
    if constexpr (ISE) {
      *(u32x2*)((char*)out + tok * 1280 + (n0 + 16 * t + 4 * G) * 2) = y;
    } else {
      *(u32x2*)(ws + OFF_H + hoff + t * 1024) = y;
      float v0 = blo(y[0]), v1 = bhi(y[0]), v2 = blo(y[1]), v3 = bhi(y[1]);
      sum += (v0 + v1) + (v2 + v3);
      sq += (v0 * v0 + v1 * v1) + (v2 * v2 + v3 * v3);
    }
  }
  if constexpr (!ISE) {
    sum += __shfl_xor(sum, 16); sq += __shfl_xor(sq, 16);
    sum += __shfl_xor(sum, 32); sq += __shfl_xor(sq, 32);
    if (l < 16) {
      f32x2 st = {sum, sq};
      *(f32x2*)(ws + OFF_ST + (tok * 8 + (j & 7)) * 8) = st;
    }
  }
}

__global__ __launch_bounds__(512) void k_ffn1(const u16* __restrict__ wg, const u16* __restrict__ wu, char* __restrict__ ws) {
  lds_t sm = (lds_t)smem_g;
  const int b = blockIdx.x, g = b & 7, j = b >> 3;
  const int wv = __builtin_amdgcn_readfirstlane(threadIdx.x >> 6), l = threadIdx.x & 63, h = l >> 5, r = l & 31;
  {
    const char* src[3];
#pragma unroll
    for (int i = 0; i < 3; i++) {
      int ci = 64 * i + l, rr = (ci * 5462) >> 16, c = ci - 12 * rr;
      src[i] = (c < 6 ? (const char*)wg + c * 16 : (const char*)wu + (c - 6) * 16) + (16 * wv + rr) * 3072 + j * 96;
    }
#pragma unroll
    for (int u = 0; u < 5; u++) {
#pragma unroll
      for (int i = 0; i < 3; i++) gldsv((unsigned)(uintptr_t)sm + (u * 8 + wv) * 3072 + i * 1024, src[i] + u * 128 * 3072);
    }
  }
  const int T = 8 * g + wv;
  const char* ab = ws + OFF_H + (size_t)T * 40 * 1024;
  const unsigned al = l * 16;
  u32x4 a[40];
#define LDH(s) { gld4<0>(a[4 * (s)], ab + (s) * 4096, al); gld4<1024>(a[4 * (s) + 1], ab + (s) * 4096, al); \
    gld4<2048>(a[4 * (s) + 2], ab + (s) * 4096, al); gld4<3072>(a[4 * (s) + 3], ab + (s) * 4096, al); }
  u32x4 st[2];
  const int tok = 32 * T + r;
  gld4<0>(st[0], ws + OFF_ST, tok * 64 + h * 32); gld4<16>(st[1], ws + OFF_ST, tok * 64 + h * 32);
  LDH(0) LDH(1) LDH(2) LDH(3) LDH(4)
  asm volatile("s_waitcnt vmcnt(20)\n\ts_barrier" : "+v"(st[0]), "+v"(st[1])::"memory");
  const int G = l >> 4, cg = l & 3, pc = ((cg & 1) << 1) | (cg >> 1);
  lds_t wp = sm + ((8 * h + ((l & 15) >> 2)) * 192 + (G & 1) * 96 + pc * 8);
  constexpr int CSP = 122880, CSF = CSP + 8 * 384;
  {
    const bf16x8 sel = selA(l);
    f32x4 c3[3] = {};
    lds_t p = wp + wv * (5 * 3072);
#pragma unroll
    for (int k = 0; k < 5; k++)
#pragma unroll
      for (int t = 0; t < 3; t++) c3[t] = mm16(sel, trd2(p + k * 3072 + t * 32, p + k * 3072 + t * 32 + 768), c3[t]);
    if (l < 16) {
      int f = 4 * ((((l >> 2) & 1) << 1) | (l >> 3)) + (l & 3);
#pragma unroll
      for (int t = 0; t < 3; t++) {
        stsf(sm + CSP + wv * 384 + (16 * t + f) * 4, c3[t][0]);
        stsf(sm + CSP + wv * 384 + 192 + (16 * t + f) * 4, c3[t][1]);
      }
    }
  }
  f32x16 acc[3] = {};
#define DSTEP(ks) _Pragma("unroll") for (int t = 0; t < 3; t++) acc[t] = mm32(trd2(wp + (ks) * 3072 + t * 32, wp + (ks) * 3072 + t * 32 + 768), B8(a[ks]), acc[t]);
#define DGRP(s, n) { wait_vm4<n>(a[4 * (s)], a[4 * (s) + 1], a[4 * (s) + 2], a[4 * (s) + 3]); \
    DSTEP(4 * (s)) DSTEP(4 * (s) + 1) DSTEP(4 * (s) + 2) DSTEP(4 * (s) + 3) }
  DGRP(0, 16) SB(); LDH(5)
  bar_lgkm();
  DGRP(1, 16) SB(); LDH(6)
  DGRP(2, 16) SB(); LDH(7)
  DGRP(3, 16) SB(); LDH(8)
  DGRP(4, 16) SB(); LDH(9)
  DGRP(5, 16) DGRP(6, 12) DGRP(7, 8) DGRP(8, 4) DGRP(9, 0)
  if (l < 48) {
    f32x2 s2 = {0.f, 0.f};
#pragma unroll
    for (int u = 0; u < 8; u++) s2 += *(__attribute__((address_space(3))) f32x2*)(sm + CSP + u * 384 + l * 8);
    *(__attribute__((address_space(3))) f32x2*)(sm + CSF + wv * 384 + l * 8) = s2;
  }
  asm volatile("" ::: "memory");
  float sum, sq;
  {
    f32x4 s0 = __builtin_bit_cast(f32x4, st[0]), s1 = __builtin_bit_cast(f32x4, st[1]);
    sum = (s0[0] + s0[2]) + (s1[0] + s1[2]);
    sq = (s0[1] + s0[3]) + (s1[1] + s1[3]);
    sum += xor32(sum); sq += xor32(sq);
  }
  const float mean = sum * (1.f / 640.f);
  const float rs = 1.f / sqrtf(fmaxf(sq * (1.f / 640.f) - mean * mean, 0.f) + 1e-5f), bb = -mean * rs;
  char* ob = ws + OFF_ACT + (size_t)(tok >> 4) * 48 * 1024 + (tok & 15) * 16;
#pragma unroll
  for (int t = 0; t < 3; t++) {
    lds_t cp = sm + (CSF + wv * 384 + (16 * t + 8 * h) * 4);
    f32x4 g0 = ldsf4(cp), g1 = ldsf4(cp + 16), u0 = ldsf4(cp + 192), u1 = ldsf4(cp + 208);
    float y[8];
#pragma unroll
    for (int e = 0; e < 8; e++) {
      float gv = rs * acc[t][e] + bb * (e < 4 ? g0[e & 3] : g1[e & 3]);
      float uv = rs * acc[t][8 + e] + bb * (e < 4 ? u0[e & 3] : u1[e & 3]);
      float z = gv * (-2.302208198f + -0.1029432397f * gv * gv);
      float e2 = __builtin_amdgcn_exp2f(z);
      y[e] = gv * uv * __builtin_amdgcn_rcpf(1.f + e2);
    }
    u32x4 o = {pk(y[0], y[1]), pk(y[2], y[3]), pk(y[4], y[5]), pk(y[6], y[7])};
    int f = 48 * j + 16 * t + 8 * h;
    *(u32x4*)(ob + (f >> 5) * 1024 + ((f >> 3) & 3) * 256) = o;
  }
}

extern "C" void kernel_launch(void* const* d_in, const int* in_sizes, int n_in, void* d_out, int out_size, void* d_ws, size_t ws_size,
                              hipStream_t stream) {
  const u16* x = (const u16*)d_in[0];
  const u16* wqkv = (const u16*)d_in[1];
  const u16* wo = (const u16*)d_in[2];
  const u16* wg = (const u16*)d_in[4];
  const u16* wu = (const u16*)d_in[5];
  const u16* wd = (const u16*)d_in[6];
  char* ws = (char*)d_ws;
  constexpr int LA = 131072 + 1536, LB = 2 * SLOT, LC = 163840, LD = 122880 + 16 * 384;
  (void)hipFuncSetAttribute((const void*)k_qkv, hipFuncAttributeMaxDynamicSharedMemorySize, LA);
  (void)hipFuncSetAttribute((const void*)k_ce<false>, hipFuncAttributeMaxDynamicSharedMemorySize, LC);
  (void)hipFuncSetAttribute((const void*)k_ce<true>, hipFuncAttributeMaxDynamicSharedMemorySize, LC);
  (void)hipFuncSetAttribute((const void*)k_ffn1, hipFuncAttributeMaxDynamicSharedMemorySize, LD);
  k_qkv<<<256, 512, LA, stream>>>(x, wqkv, ws);
  k_attn<<<256, 512, LB, stream>>>(ws);
  k_ce<false><<<256, 512, LC, stream>>>(wo, x, ws, nullptr);
  k_ffn1<<<256, 512, LD, stream>>>(wg, wu, ws);
  k_ce<true><<<256, 512, LC, stream>>>(wd, nullptr, ws, (u16*)d_out);
}
```

```cpp
#include <hip/hip_runtime.h>
#include <stdint.h>

#define EXPV 0
#define DI __device__ __forceinline__
typedef unsigned short u16;
typedef float f32x2 __attribute__((ext_vector_type(2)));
typedef float f32x4 __attribute__((ext_vector_type(4)));
typedef float f32x8 __attribute__((ext_vector_type(8)));
typedef float f32x16 __attribute__((ext_vector_type(16)));
typedef float f32x32 __attribute__((ext_vector_type(32)));
typedef __bf16 bf16x2 __attribute__((ext_vector_type(2)));
typedef __bf16 bf16x8 __attribute__((ext_vector_type(8)));
typedef __bf16 bf16x16 __attribute__((ext_vector_type(16)));
typedef short s16x4 __attribute__((ext_vector_type(4)));
typedef short s16x8 __attribute__((ext_vector_type(8)));
typedef unsigned u32x2 __attribute__((ext_vector_type(2)));
typedef unsigned u32x4 __attribute__((ext_vector_type(4)));
typedef __attribute__((address_space(3))) char* lds_t;

constexpr size_t OFF_Q = 0;
constexpr size_t OFF_KV = 3u << 20;
constexpr size_t OFF_AT = 9u << 20;
constexpr size_t OFF_H = 12u << 20;
constexpr size_t OFF_ST = 15u << 20;
constexpr size_t OFF_ACT = 16u << 20;
constexpr int VH = 352, VS = 704;
constexpr int PKT = 2560 + 4 * VS, KVH = 32 * PKT, QH = 64 * 1280;

#define SB() __builtin_amdgcn_sched_barrier(0)
#define TS0()
#define TS(I)
#define TSW(K)

DI void glds(unsigned ldsaddr, const void* sbase, unsigned voff) {
  asm volatile("s_mov_b32 m0, %0\n\ts_nop 0\n\tglobal_load_lds_dwordx4 %1, %2" ::"s"(ldsaddr), "v"(voff), "s"(sbase) : "memory");
}
DI void gldsv(unsigned ldsaddr, const void* vaddr) {
  asm volatile("s_mov_b32 m0, %0\n\ts_nop 0\n\tglobal_load_lds_dwordx4 %1, off" ::"s"(ldsaddr), "v"(vaddr) : "memory");
}
template <int IMM> DI void gld4(u32x4& d, const void* sbase, unsigned voff) {
  asm volatile("global_load_dwordx4 %0, %1, %2 offset:%c3" : "=v"(d) : "v"(voff), "s"(sbase), "i"(IMM) : "memory");
}
template <int IMM> DI void gld2(u32x2& d, const void* sbase, unsigned voff) {
  asm volatile("global_load_dwordx2 %0, %1, %2 offset:%c3" : "=v"(d) : "v"(voff), "s"(sbase), "i"(IMM) : "memory");
}
template <int N> DI void wait_vm() { asm volatile("s_waitcnt vmcnt(%c0)" ::"i"(N) : "memory"); }
template <int N> DI void wait_vm4(u32x4& a, u32x4& b, u32x4& c, u32x4& d) {
  asm volatile("s_waitcnt vmcnt(%c4)" : "+v"(a), "+v"(b), "+v"(c), "+v"(d) : "i"(N) : "memory");
}
DI void bar() { asm volatile("s_barrier" ::: "memory"); }
DI void bar_lgkm() { asm volatile("s_waitcnt lgkmcnt(0)\n\ts_barrier" ::: "memory"); }
DI u32x4 lds16(lds_t p) { return *(__attribute__((address_space(3))) u32x4*)p; }
DI u32x2 lds8(lds_t p) { return *(__attribute__((address_space(3))) u32x2*)p; }
DI f32x4 ldsf4(lds_t p) { return *(__attribute__((address_space(3))) f32x4*)p; }
DI float ldsf(lds_t p) { return *(__attribute__((address_space(3))) float*)p; }
DI void stsf(lds_t p, float v) { *(__attribute__((address_space(3))) float*)p = v; }
DI void stsf4(lds_t p, f32x4 v) { *(__attribute__((address_space(3))) f32x4*)p = v; }
DI s16x4 trd(lds_t p) { return __builtin_amdgcn_ds_read_tr16_b64_v4i16((__attribute__((address_space(3))) s16x4*)p); }
DI bf16x8 trd2(lds_t p0, lds_t p1) {
  s16x4 a = trd(p0), b = trd(p1);
  return __builtin_bit_cast(bf16x8, __builtin_shufflevector(a, b, 0, 1, 2, 3, 4, 5, 6, 7));
}
DI bf16x8 B8(u32x4 v) { return __builtin_bit_cast(bf16x8, v); }
DI unsigned pk(float a, float b) {
  f32x2 t = {a, b};
  return __builtin_bit_cast(unsigned, __builtin_convertvector(t, bf16x2));
}
DI float blo(unsigned v) { return __builtin_bit_cast(float, v << 16); }
DI float bhi(unsigned v) { return __builtin_bit_cast(float, v & 0xffff0000u); }
DI void vadd(float& a, float b) { asm("v_add_f32 %0, %0, %1" : "+v"(a) : "v"(b)); }
DI void vsq(float& a, float b) { asm("v_fmac_f32 %0, %1, %1" : "+v"(a) : "v"(b)); }
template <int C> DI float dppadd(float v) { return v + __builtin_bit_cast(float, __builtin_amdgcn_update_dpp(0, __builtin_bit_cast(int, v), C, 0xf, 0xf, true)); }
DI f32x16 mm32(bf16x8 a, bf16x8 b, f32x16 c) { return __builtin_amdgcn_mfma_f32_32x32x16_bf16(a, b, c, 0, 0, 0); }
DI f32x4 mm16(bf16x8 a, bf16x8 b, f32x4 c) { return __builtin_amdgcn_mfma_f32_16x16x32_bf16(a, b, c, 0, 0, 0); }
DI bf16x8 selA(int l) {
  unsigned v = ((l & 15) == ((l >> 4) & 1)) ? 0x3f803f80u : 0u;
  u32x4 t = {v, v, v, v};
  return B8(t);
}

extern __shared__ __attribute__((aligned(16))) char smem_g[];

struct FrA { bf16x8 x0, x1, w0, w1; };
template <bool VT> DI void a_consume(lds_t sm, int wm, int wn, int l, f32x16 (&acc)[2][2], f32x4& cs) {
  const int h = l >> 5, r = l & 31, swz = (r >> 1) & 7;
  unsigned xa[4];
#pragma unroll
  for (int kk = 0; kk < 4; kk++) xa[kk] = (64 * wm + r) * 128 + (((2 * kk + h) ^ swz) << 4);
  const unsigned wa = 16384 + (h * 4 + 2 * wn) * 512 + ((l & 15) >> 2) * 64 + ((l >> 4) & 1) * 32 + (l & 3) * 8;
  const bf16x8 sel = selA(l);
  auto rd = [&](unsigned so, int kk) {
    FrA f;
    lds_t px = sm + (xa[kk] + so), pw = sm + (wa + so) + kk * 4096;
    f.x0 = B8(lds16(px));
    f.w0 = trd2(pw, pw + 256);
    f.x1 = B8(lds16(px + 4096));
    f.w1 = trd2(pw + 512, pw + 768);
    return f;
  };
  auto m1 = [&](const FrA& f) { acc[0][0] = VT ? mm32(f.x0, f.w0, acc[0][0]) : mm32(f.w0, f.x0, acc[0][0]); };
  auto m2 = [&](const FrA& f) {
    acc[1][0] = VT ? mm32(f.x1, f.w0, acc[1][0]) : mm32(f.w0, f.x1, acc[1][0]);
    acc[0][1] = VT ? mm32(f.x0, f.w1, acc[0][1]) : mm32(f.w1, f.x0, acc[0][1]);
    acc[1][1] = VT ? mm32(f.x1, f.w1, acc[1][1]) : mm32(f.w1, f.x1, acc[1][1]);
    cs = mm16(sel, wm ? f.w1 : f.w0, cs);
  };
  bar();
  FrA cur = rd(0, 0);
#pragma unroll 1
  for (int s = 0; s < 10; s++) {
    const unsigned so = (s & 3) << 15, sn = ((s + 1) & 3) << 15;
    FrA n1 = rd(so, 1); SB(); m1(cur); m2(cur); SB();
    FrA n2 = rd(so, 2); SB(); m1(n1); m2(n1); SB();
    FrA n3 = rd(so, 3); SB(); m1(n2); m2(n2); SB();
    m1(n3); SB();
    bar_lgkm();
    cur = rd(sn, 0); SB();
    m2(n3); SB();
  }
}

__global__ __launch_bounds__(512) void k_qkv(const u16* __restrict__ x, const u16* __restrict__ w, char* __restrict__ ws) {
  TS0();
  lds_t sm = (lds_t)smem_g;
  const int b = blockIdx.x, g = b & 7, j = b >> 3;
  const int mt = 4 * (g >> 1) + (j & 3), nt = 8 * (g & 1) + (j >> 2);
  if (nt >= 15) return;
  const int wv = __builtin_amdgcn_readfirstlane(threadIdx.x >> 6), l = threadIdx.x & 63;
  constexpr int SA = 131072, SBB = SA + 512, SC = SA + 1024;
  if (wv >= 4) {
    const int hw = wv - 4;
    unsigned xo[4], wo[4];
#pragma unroll
    for (int i = 0; i < 4; i++) {
      int row = 32 * hw + 8 * i + (l >> 3);
      xo[i] = row * 1280 + (((l & 7) ^ ((row >> 1) & 7)) << 4);
      int k8 = 2 * hw + (i >> 1), u = i & 1;
      wo[i] = (8 * k8 + ((l >> 2) & 7)) * 3840 + u * 128 + (l >> 5) * 64 + (l & 3) * 16;
    }
    const char* xb = (const char*)x + (size_t)mt * 128 * 1280;
    const char* wb = (const char*)w + nt * 256;
    auto issue = [&](int s) {
      unsigned so = (unsigned)(uintptr_t)sm + ((s & 3) << 15) + hw * 4096;
      const char* xs = xb + s * 128;
      const char* wsrc = wb + (size_t)s * 64 * 3840;
#pragma unroll
      for (int i = 0; i < 4; i++) glds(so + i * 1024, xs, xo[i]);
#pragma unroll
      for (int i = 0; i < 4; i++) glds(so + 16384 + i * 1024, wsrc, wo[i]);
    };
    issue(0); issue(1); issue(2); issue(3);
    TS(1);
    wait_vm<24>(); bar();
    TS(2);
    float sum[4] = {0, 0, 0, 0}, sq[4] = {0, 0, 0, 0};
#pragma unroll 1
    for (int s = 0; s < 10; s++) {
      lds_t p = sm + (((s & 3) << 15) + hw * 4096 + l * 16);
#pragma unroll
      for (int i = 0; i < 4; i++) {
        u32x4 v = lds16(p + i * 1024);
#pragma unroll
        for (int e = 0; e < 4; e++) {
          float a = blo(v[e]), c = bhi(v[e]);
          vadd(sum[i], a); vsq(sq[i], a);
          vadd(sum[i], c); vsq(sq[i], c);
        }
      }
      if (s == 9) break;
      if (s < 7) wait_vm<16>(); else if (s == 7) wait_vm<8>(); else wait_vm<0>();
      bar_lgkm();
      if (s < 6) issue(s + 4);
      if (s == 3) TS(3);
    }
    TS(4);
#pragma unroll
    for (int i = 0; i < 4; i++) {
      float a = dppadd<0x141>(dppadd<0x4e>(dppadd<0xb1>(sum[i]))), q = dppadd<0x141>(dppadd<0x4e>(dppadd<0xb1>(sq[i])));
      float mean = a * (1.f / 640.f);
      float var = fmaxf(q * (1.f / 640.f) - mean * mean, 0.f);
      float rs = __builtin_amdgcn_rsqf(var + 1e-5f);
      if ((l & 7) == 0) {
        int row = 32 * hw + 8 * i + (l >> 3);
        stsf(sm + SA + row * 4, rs);
        stsf(sm + SBB + row * 4, -mean * rs);
      }
    }
    TS(5);
    bar();
    bar_lgkm();
    TSW(0);
    return;
  }
  const int wm = wv & 1, wn = wv >> 1, h = l >> 5, r = l & 31;
  f32x16 acc[2][2] = {};
  f32x4 cs = {};
  const bool vt = nt >= 10;
  if (vt) a_consume<true>(sm, wm, wn, l, acc, cs); else a_consume<false>(sm, wm, wn, l, acc, cs);
  TS(4);
  if (l < 16) {
    stsf(sm + SC + (64 * wn + 32 * wm + l) * 4, cs[0]);
    stsf(sm + SC + (64 * wn + 32 * wm + 16 + l) * 4, cs[1]);
  }
  bar_lgkm();
  TS(5);
  if (!vt) {
    const bool isq = nt < 5;
    const float sc = isq ? 0.22360679774997896f * 1.4426950408889634f : 1.f;
    const int hs = isq ? QH : KVH;
    char* base = ws + (isq ? OFF_Q : OFF_KV);
    const int c0 = 128 * nt - (isq ? 0 : 640) + 64 * wn + 4 * h;
#pragma unroll
    for (int mi = 0; mi < 2; mi++) {
      const int tl = 64 * wm + 32 * mi + r;
      const float a = ldsf(sm + SA + tl * 4) * sc, bb = ldsf(sm + SBB + tl * 4) * sc;
      const int T = 4 * mt + 2 * wm + mi;
      int om, orr;
      if (isq) { om = T * 1280 + r * 16; orr = T * 1280 + 1024 + r * 8; }
      else {
        int rho = (r & 19) | ((r & 4) << 1) | ((r & 8) >> 1);
        int p = ((T >> 1) - 1) & 31;
        om = p * PKT + mi * 1024 + rho * 16; orr = p * PKT + 2048 + (mi * 32 + rho) * 8;
      }
#pragma unroll
      for (int ni = 0; ni < 2; ni++) {
#pragma unroll
        for (int i = 0; i < 4; i++) {
          f32x4 c4 = ldsf4(sm + SC + (64 * wn + 32 * ni + 8 * i + 4 * h) * 4);
          int c = c0 + 32 * ni + 8 * i;
          int head = (c * 3277) >> 16, d = c - 20 * head;
          int off = head * hs + (d < 16 ? om + (d >> 3) * 512 + (d & 4) * 2 : orr);
          const f32x16& A = acc[mi][ni];
          u32x2 o;
          o[0] = pk(a * A[4 * i] + bb * c4[0], a * A[4 * i + 1] + bb * c4[1]);
          o[1] = pk(a * A[4 * i + 2] + bb * c4[2], a * A[4 * i + 3] + bb * c4[3]);
          *(u32x2*)(base + off) = o;
        }
      }
    }
  } else {
    char* base = ws + OFF_KV + (2 * mt + wm) * PKT + 2560 + h * VH;
#pragma unroll
    for (int ni = 0; ni < 2; ni++) {
      const int c = 128 * nt - 1280 + 64 * wn + 32 * ni + r;
      const int head = (c * 3277) >> 16, d = c - 20 * head;
      const float cv = ldsf(sm + SC + (64 * wn + 32 * ni + r) * 4);
      char* pb = base + head * KVH + d * 16;
#pragma unroll
      for (int mi = 0; mi < 2; mi++) {
        const f32x16& A = acc[mi][ni];
#pragma unroll
        for (int s = 0; s < 2; s++) {
          u32x4 o;
#pragma unroll
          for (int u = 0; u < 2; u++) {
            int i = 2 * s + u, tl = 64 * wm + 32 * mi + 8 * i + 4 * h;
            f32x4 a4 = ldsf4(sm + SA + tl * 4), b4 = ldsf4(sm + SBB + tl * 4);
            o[2 * u] = pk(a4[0] * A[4 * i] + b4[0] * cv, a4[1] * A[4 * i + 1] + b4[1] * cv);
            o[2 * u + 1] = pk(a4[2] * A[4 * i + 2] + b4[2] * cv, a4[3] * A[4 * i + 3] + b4[3] * cv);
          }
          *(u32x4*)(pb + VS * (2 * mi + s)) = o;
          if (d == 0) {
            u32x4 one = {0x3f803f80u, 0x3f803f80u, 0x3f803f80u, 0x3f803f80u};
            *(u32x4*)(pb + VS * (2 * mi + s) + 320) = one;
            u32x4 zero = {0, 0, 0, 0};
            *(u32x4*)(pb + VS * (2 * mi + s) + 336) = zero;
          }
        }
      }
    }
  }
  TSW(0);
}

DI f32x32 cat16(f32x16 a, f32x16 b) {
  return __builtin_shufflevector(a, b, 0, 1, 2, 3, 4, 5, 6, 7, 8, 9, 10, 11, 12, 13, 14, 15, 16, 17, 18, 19, 20, 21, 22, 23, 24, 25, 26, 27, 28, 29, 30, 31);
}
template <int O> DI bf16x16 sub8(const f32x32& s) {
  f32x8 t = __builtin_shufflevector(s, s, O, O + 1, O + 2, O + 3, O + 4, O + 5, O + 6, O + 7);
  return __builtin_bit_cast(bf16x16, t);
}
DI f32x16 smf(bf16x8 a, bf16x16 b, f32x16 c) {
  return __builtin_amdgcn_smfmac_f32_32x32x32_bf16(a, b, c, 0xDDDD, 0, 0); }
DI f32x32 res2(u32x2 a, u32x2 b, f32x32 c) {
  return __builtin_amdgcn_mfma_f32_32x32x4bf16_1k(__builtin_bit_cast(s16x4, a), __builtin_bit_cast(s16x4, b), c, 0, 0, 0);
}
template <int O> DI void exp8(f32x32& s) {
#pragma unroll
  for (int i = O; i < O + 8; i++) s[i] = __builtin_amdgcn_exp2f(s[i]);
}
DI float xor32(float v) { return __shfl_xor(v, 32); }

constexpr int SLOT = 21504;
template <int O, int N> DI void expn(f32x32& s) {
#pragma unroll
  for (int i = O; i < O + N; i++) s[i] = __builtin_amdgcn_exp2f(s[i]);
}
DI void attn_step(lds_t kp, lds_t rp, lds_t vp, bf16x8 q, u32x2 qr, const f32x16& negm, f32x32& P, f32x32& N, f32x16& O, bf16x8& v3) {
  bf16x8 k0 = B8(lds16(kp)), k1 = B8(lds16(kp + 1024));
  bf16x8 v0 = B8(lds16(vp));
  u32x2 kr = lds8(rp);
  bf16x8 v1 = B8(lds16(vp + VS)), v2 = B8(lds16(vp + 2 * VS)), v3n = B8(lds16(vp + 3 * VS));
  SB();
  expn<0, 4>(P); SB();
  O = smf(v3, sub8<24>(N), O); SB();
  expn<4, 4>(P); SB();
  f32x16 lo = mm32(k0, q, negm); SB();
  expn<8, 4>(P); SB();
  f32x16 hi = mm32(k1, q, negm); SB();
  expn<12, 4>(P); SB();
  O = smf(v0, sub8<0>(P), O); SB();
  expn<16, 4>(P); SB();
  N = res2(kr, qr, cat16(lo, hi)); SB();
  expn<20, 8>(P); SB();
  O = smf(v1, sub8<8>(P), O); SB();
  expn<28, 4>(P); SB();
  O = smf(v2, sub8<16>(P), O); SB();
  v3 = v3n;
}

__global__ __launch_bounds__(512) void k_attn(char* __restrict__ ws) {
  TS0();
  lds_t sm = (lds_t)smem_g;
  const int b = blockIdx.x, g = b & 7, j = b >> 3;
  const int head = 4 * g + (j >> 3), qb = j & 7;
  const int wv = __builtin_amdgcn_readfirstlane(threadIdx.x >> 6), l = threadIdx.x & 63, h = l >> 5, r = l & 31;
  const char* kv = ws + OFF_KV + (size_t)head * KVH;
  const int T = 8 * qb + wv;
  const char* qp = ws + OFF_Q + (size_t)head * QH + T * 1280;
  u32x4 qv, k0v, k1v; u32x2 qr, krv;
  gld4<0>(qv, qp, l * 16);
  gld2<1024>(qr, qp, r * 8);
  gld4<0>(k0v, kv + 31 * PKT, l * 16);
  gld4<1024>(k1v, kv + 31 * PKT, l * 16);
  gld2<2048>(krv, kv + 31 * PKT, l * 8);
  auto issue = [&](int c) {
    unsigned so = (unsigned)(uintptr_t)sm + (c & 1) * SLOT + wv * 1024;
    const char* src = kv + c * (4 * PKT) + wv * 1024;
    glds(so, src, l * 16);
    glds(so + 8192, src + 8192, l * 16);
    if (wv < 5) glds(so + 16384, src + 16384, l * 16);
  };
  issue(0);
  asm volatile("s_waitcnt vmcnt(2)" : "+v"(qv), "+v"(qr), "+v"(k0v), "+v"(k1v), "+v"(krv)::"memory");
  TS(1);
  const bf16x8 q = B8(qv);
  f32x32 P, N = {};
  f32x16 negm, O = {};
  bf16x8 v3 = {};
  {
    f32x16 z = {};
    P = res2(krv, qr, cat16(mm32(B8(k0v), q, z), mm32(B8(k1v), q, z)));
    float m = P[0];
#pragma unroll
    for (int i = 1; i < 32; i++) m = fmaxf(m, P[i]);
    m = fmaxf(m, xor32(m));
#pragma unroll
    for (int i = 0; i < 32; i++) P[i] -= m;
#pragma unroll
    for (int i = 0; i < 16; i++) negm[i] = -m;
  }
  const int vrow = r <= 20 ? r : 21;
  const unsigned ka = l * 16, ra = 2048 + l * 8, va = 2560 + h * VH + vrow * 16;
#pragma unroll 1
  for (int c = 0; c < 8; c++) {
    asm volatile("s_waitcnt vmcnt(0) lgkmcnt(0)\n\ts_barrier" ::: "memory");
    if (c == 0) TS(2);
    if (c == 1) TS(3);
    if (c < 7) issue(c + 1);
    const unsigned so = (c & 1) * SLOT;
    lds_t kp = sm + (ka + so), rp = sm + (ra + so), vp = sm + (va + so);
    attn_step(kp, rp, vp, q, qr, negm, P, N, O, v3);
    attn_step(kp + PKT, rp + PKT, vp + PKT, q, qr, negm, N, P, O, v3);
    attn_step(kp + 2 * PKT, rp + 2 * PKT, vp + 2 * PKT, q, qr, negm, P, N, O, v3);
    attn_step(kp + 3 * PKT, rp + 3 * PKT, vp + 3 * PKT, q, qr, negm, N, P, O, v3);
  }
  O = smf(v3, sub8<24>(N), O);
  TS(4);
  float ls = xor32(O[8]); ls = h ? O[8] : ls;
  float mag = 0.f;
#pragma unroll
  for (int i = 0; i < 8; i++) mag = fmaxf(mag, fabsf(O[i]));
  if (!h) {
#pragma unroll
    for (int i = 8; i < 12; i++) mag = fmaxf(mag, fabsf(O[i]));
  }
  bool bad = !(ls < 1e30f) || !(mag < 3e38f);
  if (__builtin_amdgcn_ballot_w64(bad) != 0) {
    float m = -INFINITY;
    O = f32x16{};
#pragma unroll 1
    for (int i = 0; i < 32; i++) {
      const char* pk_ = kv + ((i + 31) & 31) * PKT;
      const char* pv_ = kv + i * PKT + va;
      f32x16 z = {};
      f32x32 S = res2(*(const u32x2*)(pk_ + ra), qr, cat16(mm32(B8(*(const u32x4*)(pk_ + ka)), q, z), mm32(B8(*(const u32x4*)(pk_ + ka + 1024)), q, z)));
      float mn = m;
#pragma unroll
      for (int t = 0; t < 32; t++) mn = fmaxf(mn, S[t]);
      mn = fmaxf(mn, xor32(mn));
      float al = __builtin_amdgcn_exp2f(m - mn);
      m = mn;
#pragma unroll
      for (int t = 0; t < 32; t++) S[t] = __builtin_amdgcn_exp2f(S[t] - mn);
#pragma unroll
      for (int t = 0; t < 16; t++) O[t] *= al;
      O = smf(B8(*(const u32x4*)(pv_)), sub8<0>(S), O);
      O = smf(B8(*(const u32x4*)(pv_ + VS)), sub8<8>(S), O);
      O = smf(B8(*(const u32x4*)(pv_ + 2 * VS)), sub8<16>(S), O);
      O = smf(B8(*(const u32x4*)(pv_ + 3 * VS)), sub8<24>(S), O);
    }
    ls = xor32(O[8]); ls = h ? O[8] : ls;
  }
  const float inv = 1.f / ls;
  const int tok = 32 * T + r;
  char* ob = ws + OFF_AT + (size_t)(tok >> 4) * 20 * 1024 + (tok & 15) * 16;
#pragma unroll
  for (int i = 0; i < 3; i++) {
    int k = head * 20 + 8 * i + 4 * h;
    u32x2 o;
    o[0] = pk(O[4 * i] * inv, O[4 * i + 1] * inv);
    o[1] = pk(O[4 * i + 2] * inv, O[4 * i + 3] * inv);
    if (i < 2 || !h) *(u32x2*)(ob + (k >> 5) * 1024 + ((k >> 3) & 3) * 256 + (k & 4) * 2) = o;
  }
  TSW(1);
}

template <int NKB> DI void ce_mma(lds_t wp, const u32x4* a, f32x4 (&acc)[5], lds_t wp2) {
#pragma unroll
  for (int kb = 0; kb < NKB; kb++) {
#pragma unroll
    for (int t = 0; t < 5; t++) {
      bf16x8 wf = trd2(wp + kb * 5120 + t * 32, wp2 + kb * 5120 + t * 32);
      acc[t] = mm16(wf, B8(a[kb]), acc[t]);
    }
  }
}
template <bool ISE> __global__ __launch_bounds__(512) void k_ce(const u16* __restrict__ w, const u16* __restrict__ x, char* __restrict__ ws, u16* __restrict__ out) {
  TS0();
  lds_t sm = (lds_t)smem_g;
  constexpr int KBH = ISE ? 24 : 10;
  const int b = blockIdx.x, g = b & 7, j = b >> 3;
  const int row0 = 256 * g + 64 * (j >> 3), n0 = 80 * (j & 7);
  const int wv = __builtin_amdgcn_readfirstlane(threadIdx.x >> 6), l = threadIdx.x & 63;
  const int wm = wv & 3, kh = wv >> 2, G = l >> 4, tk = l & 15;
  unsigned wo[5];
#pragma unroll
  for (int i = 0; i < 5; i++) {
    int ci = 64 * i + l, rr = (ci * 6554) >> 16, c = ci - 10 * rr;
    int src = (rr & 8) ? (rr ^ 4) : rr;
    wo[i] = src * 1280 + c * 16;
  }
  const char* wb = (const char*)w + n0 * 2 + (size_t)(kh * KBH + wm) * 32 * 1280;
  auto issue = [&](int s) {
    unsigned so = (unsigned)(uintptr_t)sm + (s & 3) * 40960 + wv * 5120;
    const char* src = wb + (size_t)s * 4 * 32 * 1280;
#pragma unroll
    for (int i = 0; i < 5; i++) glds(so + i * 1024, src, wo[i]);
  };
  const char* ab = ws + (ISE ? OFF_ACT : OFF_AT) + (size_t)((row0 >> 4) + wm) * (2 * KBH) * 1024 + kh * KBH * 1024;
  u32x4 a[KBH];
  const unsigned al = l * 16;
#define LDA(s) { gld4<0>(a[4 * (s)], ab + (s) * 4096, al); gld4<1024>(a[4 * (s) + 1], ab + (s) * 4096, al); \
    if (4 * (s) + 2 < KBH) { gld4<2048>(a[(4 * (s) + 2) % KBH], ab + (s) * 4096, al); gld4<3072>(a[(4 * (s) + 3) % KBH], ab + (s) * 4096, al); } }
  const int tok = row0 + 16 * wm + tk;
  u32x2 rs[5];
  const unsigned hoff = ((tok >> 5) * 40 + (n0 >> 4)) * 1024 + ((G >> 1) * 32 + (tok & 31)) * 16 + (G & 1) * 8;
  const unsigned wr = kh * 20480 + (8 * G + (tk >> 2)) * 160 + (l & 3) * 8;
  const unsigned w0 = wr + (G & 1) * 640, w1 = wr + ((G & 1) ^ 1) * 640;
  f32x4 acc[5] = {};
  if constexpr (ISE) {
    issue(0); LDA(0) issue(1); LDA(1) issue(2); LDA(2) issue(3); LDA(3) LDA(4) LDA(5)
    gld2<0>(rs[0], ws + OFF_H, hoff); gld2<1024>(rs[1], ws + OFF_H, hoff); gld2<2048>(rs[2], ws + OFF_H, hoff);
    gld2<3072>(rs[3], ws + OFF_H, hoff); gld2<0>(rs[4], ws + OFF_H + 4096, hoff);
    TS(1);
    wait_vm4<40>(a[0], a[1], a[2], a[3]); bar();
    TS(2);
    ce_mma<4>(sm + w0, a, acc, sm + w1);
    wait_vm4<31>(a[4], a[5], a[6], a[7]); bar_lgkm(); issue(4);
    ce_mma<4>(sm + w0 + 40960, a + 4, acc, sm + w1 + 40960);
    wait_vm4<27>(a[8], a[9], a[10], a[11]); bar_lgkm(); issue(5);
    ce_mma<4>(sm + w0 + 81920, a + 8, acc, sm + w1 + 81920);
    wait_vm4<23>(a[12], a[13], a[14], a[15]); bar();
    TS(3);
    ce_mma<4>(sm + w0 + 122880, a + 12, acc, sm + w1 + 122880);
    wait_vm4<5>(a[16], a[17], a[18], a[19]); bar();
    ce_mma<4>(sm + w0, a + 16, acc, sm + w1);
    wait_vm4<0>(a[20], a[21], a[22], a[23]); bar();
    ce_mma<4>(sm + w0 + 40960, a + 20, acc, sm + w1 + 40960);
  } else {
    const unsigned xoff = tok * 1280 + (n0 + 4 * G) * 2;
    issue(0); LDA(0) issue(1); LDA(1)
    if (wm < 2) issue(2);
    LDA(2)
    gld2<0>(rs[0], x, xoff); gld2<32>(rs[1], x, xoff); gld2<64>(rs[2], x, xoff); gld2<96>(rs[3], x, xoff); gld2<128>(rs[4], x, xoff);
    TS(1);
    wait_vm4<16>(a[0], a[1], a[2], a[3]); bar();
    TS(2);
    ce_mma<4>(sm + w0, a, acc, sm + w1);
    wait_vm4<7>(a[4], a[5], a[6], a[7]); bar();
    TS(3);
    ce_mma<4>(sm + w0 + 40960, a + 4, acc, sm + w1 + 40960);
    asm volatile("s_waitcnt vmcnt(0)" : "+v"(a[8]), "+v"(a[9])::"memory"); bar();
    ce_mma<2>(sm + w0 + 81920, a + 8, acc, sm + w1 + 81920);
  }
  asm volatile("" : "+v"(rs[0]), "+v"(rs[1]), "+v"(rs[2]), "+v"(rs[3]), "+v"(rs[4]));
  TS(4);
  lds_t rb = sm + (122880 + wm * 5120 + l * 16);
  if (kh) {
#pragma unroll
    for (int t = 0; t < 5; t++) stsf4(rb + t * 1024, acc[t]);
  }
  bar_lgkm();
  TS(5);
  if (kh) { TSW(ISE ? 4 : 2); return; }
  float sum = 0.f, sq = 0.f;
#pragma unroll
  for (int t = 0; t < 5; t++) {
    f32x4 o = acc[t] + ldsf4(rb + t * 1024);
    u32x2 y;
    y[0] = pk(o[0] + blo(rs[t][0]), o[1] + bhi(rs[t][0]));
    y[1] = pk(o[2] + blo(rs[t][1]), o[3] + bhi(rs[t][1]));
    if constexpr (ISE) {
      *(u32x2*)((char*)out + tok * 1280 + (n0 + 16 * t + 4 * G) * 2) = y;
    } else {
      *(u32x2*)(ws + OFF_H + hoff + t * 1024) = y;
      float v0 = blo(y[0]), v1 = bhi(y[0]), v2 = blo(y[1]), v3 = bhi(y[1]);
      sum += (v0 + v1) + (v2 + v3);
      sq += (v0 * v0 + v1 * v1) + (v2 * v2 + v3 * v3);
    }
  }
  if constexpr (!ISE) {
    sum += __shfl_xor(sum, 16); sq += __shfl_xor(sq, 16);
    sum += __shfl_xor(sum, 32); sq += __shfl_xor(sq, 32);
    if (l < 16) {
      f32x2 st = {sum, sq};
      *(f32x2*)(ws + OFF_ST + (tok * 8 + (j & 7)) * 8) = st;
    }
  }
  TSW(ISE ? 4 : 2);
}

__global__ __launch_bounds__(512) void k_ffn1(const u16* __restrict__ wg, const u16* __restrict__ wu, char* __restrict__ ws) {
  TS0();
  lds_t sm = (lds_t)smem_g;
  const int b = blockIdx.x, g = b & 7, j = b >> 3;
  const int wv = __builtin_amdgcn_readfirstlane(threadIdx.x >> 6), l = threadIdx.x & 63, h = l >> 5, r = l & 31;
  {
    const char* src[3];
#pragma unroll
    for (int i = 0; i < 3; i++) {
      int ci = 64 * i + l, rr = (ci * 5462) >> 16, c = ci - 12 * rr;
      src[i] = (c < 6 ? (const char*)wg + c * 16 : (const char*)wu + (c - 6) * 16) + (16 * wv + rr) * 3072 + j * 96;
    }
#pragma unroll
    for (int u = 0; u < 5; u++) {
#pragma unroll
      for (int i = 0; i < 3; i++) gldsv((unsigned)(uintptr_t)sm + (u * 8 + wv) * 3072 + i * 1024, src[i] + u * 128 * 3072);
    }
  }
  const int T = 8 * g + wv;
  const char* ab = ws + OFF_H + (size_t)T * 40 * 1024;
  const unsigned al = l * 16;
  u32x4 a[40];
#define LDH(s) { gld4<0>(a[4 * (s)], ab + (s) * 4096, al); gld4<1024>(a[4 * (s) + 1], ab + (s) * 4096, al); \
    gld4<2048>(a[4 * (s) + 2], ab + (s) * 4096, al); gld4<3072>(a[4 * (s) + 3], ab + (s) * 4096, al); }
  u32x4 st[2];
  const int tok = 32 * T + r;
  gld4<0>(st[0], ws + OFF_ST, tok * 64 + h * 32); gld4<16>(st[1], ws + OFF_ST, tok * 64 + h * 32);
  LDH(0) LDH(1) LDH(2) LDH(3) LDH(4)
  TS(1);
  asm volatile("s_waitcnt vmcnt(20)\n\ts_barrier" : "+v"(st[0]), "+v"(st[1])::"memory");
  TS(2);
  const int G = l >> 4, cg = l & 3, pc = ((cg & 1) << 1) | (cg >> 1);
  lds_t wp = sm + ((8 * h + ((l & 15) >> 2)) * 192 + (G & 1) * 96 + pc * 8);
  constexpr int CSP = 122880, CSF = CSP + 8 * 384;
  {
    const bf16x8 sel = selA(l);
    f32x4 c3[3] = {};
    lds_t p = wp + wv * (5 * 3072);
#pragma unroll
    for (int k = 0; k < 5; k++)
#pragma unroll
      for (int t = 0; t < 3; t++) c3[t] = mm16(sel, trd2(p + k * 3072 + t * 32, p + k * 3072 + t * 32 + 768), c3[t]);
    if (l < 16) {
      int f = 4 * ((((l >> 2) & 1) << 1) | (l >> 3)) + (l & 3);
#pragma unroll
      for (int t = 0; t < 3; t++) {
        stsf(sm + CSP + wv * 384 + (16 * t + f) * 4, c3[t][0]);
        stsf(sm + CSP + wv * 384 + 192 + (16 * t + f) * 4, c3[t][1]);
      }
    }
  }
  f32x16 acc[3] = {};
#define DSTEP(ks) _Pragma("unroll") for (int t = 0; t < 3; t++) acc[t] = mm32(trd2(wp + (ks) * 3072 + t * 32, wp + (ks) * 3072 + t * 32 + 768), B8(a[ks]), acc[t]);
#define DGRP(s, n) { wait_vm4<n>(a[4 * (s)], a[4 * (s) + 1], a[4 * (s) + 2], a[4 * (s) + 3]); \
    DSTEP(4 * (s)) DSTEP(4 * (s) + 1) DSTEP(4 * (s) + 2) DSTEP(4 * (s) + 3) }
  DGRP(0, 16) SB(); LDH(5)
  bar_lgkm();
  TS(3);
  DGRP(1, 16) SB(); LDH(6)
  DGRP(2, 16) SB(); LDH(7)
  DGRP(3, 16) SB(); LDH(8)
  DGRP(4, 16) SB(); LDH(9)
  DGRP(5, 16) DGRP(6, 12) DGRP(7, 8) DGRP(8, 4) DGRP(9, 0)
  TS(4);
  if (l < 48) {
    f32x2 s2 = {0.f, 0.f};
#pragma unroll
    for (int u = 0; u < 8; u++) s2 += *(__attribute__((address_space(3))) f32x2*)(sm + CSP + u * 384 + l * 8);
    *(__attribute__((address_space(3))) f32x2*)(sm + CSF + wv * 384 + l * 8) = s2;
  }
  asm volatile("" ::: "memory");
  float sum, sq;
  {
    f32x4 s0 = __builtin_bit_cast(f32x4, st[0]), s1 = __builtin_bit_cast(f32x4, st[1]);
    sum = (s0[0] + s0[2]) + (s1[0] + s1[2]);
    sq = (s0[1] + s0[3]) + (s1[1] + s1[3]);
    sum += xor32(sum); sq += xor32(sq);
  }
  const float mean = sum * (1.f / 640.f);
  const float rs = 1.f / sqrtf(fmaxf(sq * (1.f / 640.f) - mean * mean, 0.f) + 1e-5f), bb = -mean * rs;
  char* ob = ws + OFF_ACT + (size_t)(tok >> 4) * 48 * 1024 + (tok & 15) * 16;
#pragma unroll
  for (int t = 0; t < 3; t++) {
    lds_t cp = sm + (CSF + wv * 384 + (16 * t + 8 * h) * 4);
    f32x4 g0 = ldsf4(cp), g1 = ldsf4(cp + 16), u0 = ldsf4(cp + 192), u1 = ldsf4(cp + 208);
    float y[8];
#pragma unroll
    for (int e = 0; e < 8; e++) {
      float gv = rs * acc[t][e] + bb * (e < 4 ? g0[e & 3] : g1[e & 3]);
      float uv = rs * acc[t][8 + e] + bb * (e < 4 ? u0[e & 3] : u1[e & 3]);
      float z = gv * (-2.302208198f + -0.1029432397f * gv * gv);
      float e2 = __builtin_amdgcn_exp2f(z);
      y[e] = gv * uv * __builtin_amdgcn_rcpf(1.f + e2);
    }
    u32x4 o = {pk(y[0], y[1]), pk(y[2], y[3]), pk(y[4], y[5]), pk(y[6], y[7])};
    int f = 48 * j + 16 * t + 8 * h;
    *(u32x4*)(ob + (f >> 5) * 1024 + ((f >> 3) & 3) * 256) = o;
  }
  TSW(3);
}

extern "C" void kernel_launch(void* const* d_in, const int* in_sizes, int n_in, void* d_out, int out_size, void* d_ws, size_t ws_size,
                              hipStream_t stream) {
  const u16* x = (const u16*)d_in[0];
  const u16* wqkv = (const u16*)d_in[1];
  const u16* wo = (const u16*)d_in[2];
  const u16* wg = (const u16*)d_in[4];
  const u16* wu = (const u16*)d_in[5];
  const u16* wd = (const u16*)d_in[6];
  char* ws = (char*)d_ws;
  constexpr int LA = 131072 + 1536, LB = 2 * SLOT, LC = 163840, LD = 122880 + 16 * 384;
  (void)hipFuncSetAttribute((const void*)k_qkv, hipFuncAttributeMaxDynamicSharedMemorySize, LA);
  (void)hipFuncSetAttribute((const void*)k_ce<false>, hipFuncAttributeMaxDynamicSharedMemorySize, LC);
  (void)hipFuncSetAttribute((const void*)k_ce<true>, hipFuncAttributeMaxDynamicSharedMemorySize, LC);
  (void)hipFuncSetAttribute((const void*)k_ffn1, hipFuncAttributeMaxDynamicSharedMemorySize, LD);
  k_qkv<<<256, 512, LA, stream>>>(x, wqkv, ws);
  k_attn<<<256, 512, LB, stream>>>(ws);
  k_ce<false><<<256, 512, LC, stream>>>(wo, x, ws, nullptr);
  k_ffn1<<<256, 512, LD, stream>>>(wg, wu, ws);
  k_ce<true><<<256, 512, LC, stream>>>(wd, nullptr, ws, (u16*)d_out);
}
```

```cpp
#define CSL2 0
#define CS0 0
#define EWT 0
#define ENS 2
#define HBD 3
#define PKL 0
#define HX1 0
#include <hip/hip_runtime.h>
#include <stdint.h>

#define EXPV 0
#define FINE 0
#define STATV 0
#define NBC 8
#define NBD 8
#define NBE 10
#define NBS 6
#define Y0Q 27
#define SPSL 100
#define EPL 3
#define EPS 0
#define HGATE 1
#define AMF 1
#define SU0 29
#define SU1 89
#define EG0 16
#define EG1 24
#define EN 24
#define EW 8
#define ESC 2
#define EA 23
#define EB 3
#define EC 2
#define ZD2 1
#define ZF2 0
#define UG 50
#define PGI 1
#define RS0 20
#define RSS 3
#define HPR 0
#define ELA 20
#define EBM 0x1ab
#define PGC 9
#define ELS 2
#define HSK 8
#define EDW 1
#define PGS 4
#define FRD 0
#define UZ 13
#define TRO 1
#define PGW 0
#define PKE 0
#define DTC 0
#define DT0 24
#define LDX 0
#define EN1 6
#define EPS2 0
#define PRK 0
#define ETCH 0
#define PKN 21
#define DWE 3
#define DF0 19
#define DF1 2
#define DP0 12
#define DSCH 0
#define DI __device__ __forceinline__
typedef unsigned short u16;
typedef float f32x2 __attribute__((ext_vector_type(2)));
typedef float f32x4 __attribute__((ext_vector_type(4)));
typedef float f32x8 __attribute__((ext_vector_type(8)));
typedef float f32x16 __attribute__((ext_vector_type(16)));
typedef __bf16 bf16x2 __attribute__((ext_vector_type(2)));
typedef __bf16 bf16x8 __attribute__((ext_vector_type(8)));
typedef __bf16 bf16x16 __attribute__((ext_vector_type(16)));
typedef short s16x4 __attribute__((ext_vector_type(4)));
typedef unsigned u32x2 __attribute__((ext_vector_type(2)));
typedef unsigned u32x4 __attribute__((ext_vector_type(4)));
typedef unsigned u32x8 __attribute__((ext_vector_type(8)));
typedef unsigned u32x16 __attribute__((ext_vector_type(16)));
typedef unsigned u32x32 __attribute__((ext_vector_type(32)));
typedef __attribute__((address_space(3))) char* lds_t;
typedef __amdgpu_buffer_rsrc_t rsrc_t;

constexpr unsigned OFF_Q = 0;
constexpr unsigned OFF_KV = 3u << 20;
constexpr unsigned OFF_AT = 9u << 20;
constexpr unsigned OFF_H = OFF_AT + 2621440;
constexpr unsigned OFF_ST = OFF_H + 2621440;
constexpr unsigned OFF_ACT = OFF_ST + 131072;
constexpr unsigned OFF_F1 = (22u << 20) + 65536;
constexpr unsigned OFF_F2 = OFF_F1 + 65536;
constexpr unsigned OFF_EP = OFF_F2 + 65536;
constexpr unsigned OFF_XS = OFF_EP + 65536;
constexpr unsigned OFF_C2 = OFF_XS + 65536;
constexpr unsigned WS_USED = 23u << 20;
constexpr int VH = 384, VS = 768;
constexpr int PKT = 2560 + 4 * VS, KVH = 32 * PKT, QH = 32 * 2560;

#define OAUX 0
#define EPP 2
#define DSP 0
#define CSP 0
#define DEP 0
#define CSL 0
#define DSL 0
#define HG 0
#define DPR 0
#define NOFIX 0
#define RARE(x) __builtin_expect(!!(x), 0)
#define SB() __builtin_amdgcn_sched_barrier(0)
#define PCNT(I)
#define RCNT(I)
#define TS0()
#define TS(I)
#define TSL(I)
#define TSA(I)
#define TSX(I)
#define TSM(I)
#define TS2(I)
#define TSW(K)

DI void glds(unsigned ldsaddr, const void* sbase, unsigned voff) {
  asm volatile("s_mov_b32 m0, %0\n\ts_nop 0\n\tglobal_load_lds_dwordx4 %1, %2" ::"s"(ldsaddr), "v"(voff), "s"(sbase) : "memory");
}
DI void glds1(unsigned ldsaddr, const void* sbase, unsigned voff) {
  asm volatile("s_mov_b32 m0, %0\n\ts_nop 0\n\tglobal_load_lds_dwordx4 %1, %2 sc1" ::"s"(ldsaddr), "v"(voff), "s"(sbase) : "memory");
}
DI void gldb(unsigned ldsaddr, rsrc_t r, unsigned voff, unsigned soff) {
  asm volatile("s_mov_b32 m0, %0\n\ts_nop 0\n\tbuffer_load_dwordx4 %1, %2, %3 offen lds" ::"s"(ldsaddr), "v"(voff), "s"(r), "s"(soff) : "memory");
}
DI void touch(unsigned ldsaddr, const void* sbase, unsigned voff, unsigned long long lanes) {
  asm volatile("s_mov_b32 m0, %0\n\ts_mov_b64 exec, %3\n\tglobal_load_lds_dword %1, %2\n\ts_mov_b64 exec, -1" ::"s"(ldsaddr), "v"(voff), "s"(sbase), "s"(lanes) : "memory");
}
DI void pin(unsigned& v) { asm volatile("" : "+v"(v)); }
DI rsrc_t mkr(const void* p, unsigned n) { return __builtin_amdgcn_make_buffer_rsrc((void*)p, 0, n, 0x00020000); }
template <int AUX> DI u32x4 bl4(rsrc_t r, unsigned v, unsigned s) { return __builtin_amdgcn_raw_buffer_load_b128(r, v, s, AUX); }
template <int AUX> DI u32x2 bl2(rsrc_t r, unsigned v, unsigned s) { return __builtin_amdgcn_raw_buffer_load_b64(r, v, s, AUX); }
template <int AUX> DI unsigned bl1(rsrc_t r, unsigned v, unsigned s) { return __builtin_amdgcn_raw_buffer_load_b32(r, v, s, AUX); }
template <int AUX> DI void bs4(u32x4 d, rsrc_t r, unsigned v, unsigned s) {
  asm("" : "+v"(d));
  __builtin_amdgcn_raw_buffer_store_b128(d, r, v, s, AUX);
  asm volatile("s_nop 1" ::"v"(d) : "memory");
}
template <int AUX> DI void bs2(u32x2 d, rsrc_t r, unsigned v, unsigned s) { __builtin_amdgcn_raw_buffer_store_b64(d, r, v, s, AUX); }
template <int AUX> DI void bs1(unsigned d, rsrc_t r, unsigned v, unsigned s) { __builtin_amdgcn_raw_buffer_store_b32(d, r, v, s, AUX); }
constexpr int DEV = 16;
DI void wait_vmn(int n) { asm volatile("s_waitcnt vmcnt(%c0)" ::"i"(n) : "memory"); }
template <int N> DI void wait_vm() { asm volatile("s_waitcnt vmcnt(%c0)" ::"i"(N) : "memory"); }
DI void bar() { asm volatile("s_barrier" ::: "memory"); }
DI void bar_lgkm() { asm volatile("s_waitcnt lgkmcnt(0)\n\ts_barrier" ::: "memory"); }
DI void fence() { asm volatile("" ::: "memory"); }
DI u32x4 lds16(lds_t p) { return *(__attribute__((address_space(3))) u32x4*)p; }
DI f32x4 ldsf4(lds_t p) { return *(__attribute__((address_space(3))) f32x4*)p; }
DI float ldsf(lds_t p) { return *(__attribute__((address_space(3))) float*)p; }
DI void stsf(lds_t p, float v) { *(__attribute__((address_space(3))) float*)p = v; }
DI void stsf4(lds_t p, f32x4 v) { *(__attribute__((address_space(3))) f32x4*)p = v; }
DI void sts16(lds_t p, u32x4 v) { *(__attribute__((address_space(3))) u32x4*)p = v; }
DI s16x4 trd(lds_t p) { return __builtin_amdgcn_ds_read_tr16_b64_v4i16((__attribute__((address_space(3))) s16x4*)p); }
DI bf16x8 trd2(lds_t p0, lds_t p1) {
  s16x4 a = trd(p0), b = trd(p1);
  return __builtin_bit_cast(bf16x8, __builtin_shufflevector(a, b, 0, 1, 2, 3, 4, 5, 6, 7));
}
DI bf16x8 B8(u32x4 v) { return __builtin_bit_cast(bf16x8, v); }
DI unsigned pk(float a, float b) {
  f32x2 t = {a, b};
  return __builtin_bit_cast(unsigned, __builtin_convertvector(t, bf16x2));
}
DI unsigned pkc(float a, float b) { return min(pk(a, b), 0xFFFFFFFEu); }
DI float blo(unsigned v) { return __builtin_bit_cast(float, v << 16); }
DI float bhi(unsigned v) { return __builtin_bit_cast(float, v & 0xffff0000u); }
DI float u2f(unsigned v) { return __builtin_bit_cast(float, v); }
DI unsigned f2u(float v) { return __builtin_bit_cast(unsigned, v); }
DI void stat2(float& s, float& q, unsigned v) {
  const bf16x2 a = __builtin_bit_cast(bf16x2, v);
  s = __builtin_amdgcn_fdot2_f32_bf16(a, __builtin_bit_cast(bf16x2, 0x3f803f80u), s, false);
  q = __builtin_amdgcn_fdot2_f32_bf16(a, a, q, false);
}
DI void vadd(float& s, float a) { asm("v_add_f32 %0, %0, %1" : "+v"(s) : "v"(a)); }
DI void vsq(float& s, float a) { asm("v_fma_f32 %0, %1, %1, %0" : "+v"(s) : "v"(a)); }
DI unsigned rfl(unsigned v) { return __builtin_amdgcn_readfirstlane(v); }
template <int C> DI float dppadd(float v) { return v + __builtin_bit_cast(float, __builtin_amdgcn_update_dpp(0, __builtin_bit_cast(int, v), C, 0xf, 0xf, true)); }
DI f32x16 mm32(bf16x8 a, bf16x8 b, f32x16 c) { return __builtin_amdgcn_mfma_f32_32x32x16_bf16(a, b, c, 0, 0, 0); }
DI f32x4 mm16(bf16x8 a, bf16x8 b, f32x4 c) { return __builtin_amdgcn_mfma_f32_16x16x32_bf16(a, b, c, 0, 0, 0); }
DI bf16x8 selA(int l) {
  unsigned v = ((l & 15) == ((l >> 4) & 1)) ? 0x3f803f80u : 0u;
  u32x4 t = {v, v, v, v};
  return B8(t);
}
DI bool anyl(bool c) { return __builtin_amdgcn_ballot_w64(c) != 0; }

extern __shared__ __attribute__((aligned(16))) char smem_g[];
extern "C" __global__ void hk_tail() ;
DI void code_prefetch(unsigned ldsaddr, int piece, int l) {
  unsigned long long pc;
  unsigned back, len;
  asm volatile("s_getpc_b64 %0\n.Lpf%=:\n\ts_mov_b32 %1, .Lpf%=-hk_main\n\ts_mov_b32 %2, hk_tail-hk_main" : "=s"(pc), "=s"(back), "=s"(len));
  const char* base = (const char*)(pc - back);
  for (unsigned o = piece * 1024; o + 1024 <= len; o += 32768) glds(ldsaddr, base + o, l * 16);
}

template <int O> DI bf16x16 sub8(const f32x16& s) {
  f32x8 t = __builtin_shufflevector(s, s, O, O + 1, O + 2, O + 3, O + 4, O + 5, O + 6, O + 7);
  return __builtin_bit_cast(bf16x16, t);
}
DI f32x16 smf(bf16x8 a, bf16x16 b, f32x16 c) { return __builtin_amdgcn_smfmac_f32_32x32x32_bf16(a, b, c, 0xDDDD, 0, 0); }
DI f32x16 mm8(u32x2 a, u32x2 b, f32x16 c) {
  return __builtin_amdgcn_mfma_f32_32x32x8bf16_1k(__builtin_bit_cast(s16x4, a), __builtin_bit_cast(s16x4, b), c, 0, 0, 0);
}
DI void swp32(unsigned& a, unsigned& c) { asm("s_nop 1\n\tv_permlane32_swap_b32 %0, %1" : "+v"(a), "+v"(c)); }
DI void swp16(unsigned& a, unsigned& c) { asm("s_nop 1\n\tv_permlane16_swap_b32 %0, %1" : "+v"(a), "+v"(c)); }
DI f32x2 swap32(float v) {
  unsigned a = f2u(v), c = a;
  swp32(a, c);
  f32x2 o = {u2f(a), u2f(c)};
  return o;
}
DI float max32(float v) { f32x2 r = swap32(v); return fmaxf(r[0], r[1]); }
DI float sum32(float v) { f32x2 r = swap32(v); return r[0] + r[1]; }
DI float sum16(float v) { unsigned a = f2u(v), c = a; swp16(a, c); return u2f(a) + u2f(c); }
DI u32x2 lo2(u32x4 v) { u32x2 r = {v[0], v[1]}; return r; }
DI u32x2 hi2(u32x4 v) { u32x2 r = {v[2], v[3]}; return r; }
DI u32x4 cat2(u32x2 a, u32x2 b) { u32x4 r = {a[0], a[1], b[0], b[1]}; return r; }
DI unsigned mx4(u32x4 v) { return max(max(v[0], v[1]), max(v[2], v[3])); }
DI unsigned neq(u32x4 f, unsigned t0, unsigned t1) { return ((f[0] ^ t0) | (f[1] ^ t1)) | ((f[2] ^ t0) | (f[3] ^ t1)); }
#define PARKE 1
DI bool isn(float v) { return v != v; }
constexpr int SLOT = 4 * PKT, L_UP = 61440, L_WO = 61440, L_WE = 122880, L_CS = L_WE + 38912, L_DUM = 2 * SLOT;
DI float max16(const f32x16& s, float m) {
#pragma unroll
  for (int i = 0; i < 16; i++) m = fmaxf(m, s[i]);
  return m;
}
DI f32x16 attn_exact(u32x4 q, u32x2 qr, rsrc_t rws, unsigned kv, unsigned ka, unsigned ra, unsigned va, int h) {
  float m = -INFINITY;
  f32x16 O = {};
  if (h) qr[0] = 0;
#pragma unroll 1
  for (int i = 0; i < 32; i++) {
    const unsigned pk_ = kv + ((i + 31) & 31) * PKT, pv_ = kv + i * PKT;
    const f32x16 z = {};
    u32x4 kr = bl4<DEV>(rws, ra, pk_);
    f32x16 lo = mm8(lo2(kr), qr, mm32(B8(bl4<DEV>(rws, ka, pk_)), B8(q), z));
    f32x16 hi = mm8(hi2(kr), qr, mm32(B8(bl4<DEV>(rws, ka + 1024, pk_)), B8(q), z));
    float mn = max32(max16(hi, max16(lo, m)));
    float al = __builtin_amdgcn_exp2f(m - mn);
    m = mn;
#pragma unroll
    for (int e = 0; e < 16; e++) { lo[e] = __builtin_amdgcn_exp2f(lo[e] - mn); hi[e] = __builtin_amdgcn_exp2f(hi[e] - mn); O[e] *= al; }
    O = smf(B8(bl4<DEV>(rws, va, pv_)), sub8<0>(lo), O);
    O = smf(B8(bl4<DEV>(rws, va + VS, pv_)), sub8<8>(lo), O);
    O = smf(B8(bl4<DEV>(rws, va + 2 * VS, pv_)), sub8<0>(hi), O);
    O = smf(B8(bl4<DEV>(rws, va + 3 * VS, pv_)), sub8<8>(hi), O);
  }
  return O;
}
DI float mag12(const f32x16& O, float mag) {
#pragma unroll
  for (int i = 0; i < 12; i++) mag = fmaxf(mag, fabsf(O[i]));
  return mag;
}
DI u32x4 fetch16(rsrc_t r, unsigned v, unsigned s, int& budget) {
  u32x4 a;
#pragma unroll 1
  do { fence(); a = bl4<DEV>(r, v, s); } while (anyl(mx4(a) == ~0u) && --budget > 0);
  return a;
}
DI u32x2 fetch8(rsrc_t r, unsigned v, unsigned s, int& budget) {
  u32x2 a;
#pragma unroll 1
  do { fence(); a = bl2<DEV>(r, v, s); } while (anyl(max(a[0], a[1]) == ~0u) && --budget > 0);
  return a;
}
DI void gate1(rsrc_t r, unsigned v, unsigned s, int& budget) {
  unsigned a;
#pragma unroll 1
  do { fence(); a = bl1<DEV>(r, v, s); } while (anyl(a == ~0u) && --budget > 0);
}
DI int lane() { int l; asm volatile("v_mbcnt_lo_u32_b32 %0, -1, 0\n\tv_mbcnt_hi_u32_b32 %0, -1, %0" : "=v"(l)); return l; }
#define LANES() const int l = lane(), h = l >> 5, r = l & 31, G = l >> 4, tk = l & 15; (void)h; (void)r; (void)G; (void)tk;
struct GelK { float k0, k1, vq; };
DI GelK gelk(float r1) { const float r2 = r1 * r1; return GelK{-2.302208198f * r1, -0.1029432397f * r1 * r2, __builtin_amdgcn_rcpf(r2)}; }
DI unsigned gel2(f32x2 ga, f32x2 ua, GelK k) {
  const f32x2 zz = ga * __builtin_elementwise_fma(ga * ga, f32x2{k.k1, k.k1}, f32x2{k.k0, k.k0});
  const f32x2 ex = {__builtin_amdgcn_exp2f(zz[0]), __builtin_amdgcn_exp2f(zz[1])};
  const f32x2 dn = __builtin_elementwise_fma(ex, f32x2{k.vq, k.vq}, f32x2{k.vq, k.vq});
  const f32x2 o = (ga * ua) * f32x2{__builtin_amdgcn_rcpf(dn[0]), __builtin_amdgcn_rcpf(dn[1])};
  return pkc(o[0], o[1]);
}
template <bool SEQ, class F> DI void act_out(F grp, rsrc_t rws, int l, unsigned ab, int fs) {
  u32x2 y[6];
#pragma unroll
  for (int i = 0; i < 6; i++) {
    y[i] = grp(i);
    if (i == 1 || i == 3) bs4<DEV>(cat2(y[i - 1], y[i]), rws, l * 16, ab + (i >> 1) * 49152 + (fs & 1) * 1024);
    if (SEQ) SB();
  }
  unsigned a0 = y[4][0], c0 = y[5][0], a1 = y[4][1], c1 = y[5][1];
  swp32(a0, c0); swp32(a1, c1);
  bs4<DEV>(u32x4{a0, a1, c0, c1}, rws, (l >> 5) * 49152 + (l & 31) * 16, ab + 2048 + (fs & 1) * 512);
}
DI void mm16i(f32x4& c, bf16x8 a, u32x4 b, bool first) {
  if (first) asm volatile("v_mfma_f32_16x16x32_bf16 %0, %1, %2, 0" : "=&v"(c) : "v"(a), "v"(b));
  else asm volatile("v_mfma_f32_16x16x32_bf16 %0, %1, %2, %0" : "+v"(c) : "v"(a), "v"(b));
}
struct Fr { s16x4 a, b; };
DI bf16x8 F8(const Fr& f) { return __builtin_bit_cast(bf16x8, __builtin_shufflevector(f.a, f.b, 0, 1, 2, 3, 4, 5, 6, 7)); }
template <int V> struct IC { static constexpr int v = V; };
constexpr int imin(int a, int b) { return a < b ? a : b; }
template <int I, int N, class F> DI void sfor(F&& f) { if constexpr (I < N) { f(IC<I>{}); sfor<I + 1, N>(f); } }
struct W5 { bf16x8 w[5]; };
struct W3 { bf16x8 w[3]; };
DI void step5(f32x4 (&acc)[2][5], W5& cur, u32x4 a0, u32x4 a1, lds_t p0, lds_t p1, bool first, bool more) {
  const f32x4 z = {};
#pragma unroll
  for (int t = 0; t < 5; t++) {
    acc[0][t] = mm16(cur.w[t], B8(a0), first ? z : acc[0][t]);
    acc[1][t] = mm16(cur.w[t], B8(a1), first ? z : acc[1][t]);
    SB();
    if (more) cur.w[t] = trd2(p0 + t * 32, p1 + t * 32);
    SB();
  }
}
DI W5 rd5(lds_t p0, lds_t p1) {
  W5 f;
#pragma unroll
  for (int t = 0; t < 5; t++) f.w[t] = trd2(p0 + t * 32, p1 + t * 32);
  return f;
}

#define HBR 1
#define ATT_ASM \
  "s_mov_b32 %[c], 2\n\ts_movk_i32 %[d], 22528\n\ts_branch 3f\n\t.p2align 6\n\t" \
  "1:\n\ts_barrier\n\t3:\n\tv_smfmac_f32_32x32x32_bf16 v[16:31], v[108:111], v[64:71], v140\n\t" \
  "ds_read_b128 v[136:139], v142 offset:0\n\tds_read_b128 v[108:111], v143 offset:0\n\tv_exp_f32 v32, v32\n\tv_exp_f32 v33, v33\n\t" \
  "v_exp_f32 v34, v34\n\tv_exp_f32 v35, v35\n\tv_mfma_f32_32x32x8_bf16 v[48:63], v[134:135], v[104:105], v[48:63]\n\tv_exp_f32 v36, v36\n\t" \
  "v_exp_f32 v37, v37\n\tv_exp_f32 v38, v38\n\tv_exp_f32 v39, v39\n\tv_smfmac_f32_32x32x32_bf16 v[16:31], v[112:115], v[72:79], v140\n\t" \
  "ds_read_b128 v[112:115], v143 offset:768\n\tv_exp_f32 v40, v40\n\tv_exp_f32 v41, v41\n\tv_exp_f32 v42, v42\n\t" \
  "v_exp_f32 v43, v43\n\tv_mfma_f32_32x32x16_bf16 v[64:79], v[124:127], v[100:103], 0\n\tds_read_b128 v[124:127], v141 offset:0\n\tv_exp_f32 v44, v44\n\t" \
  "v_exp_f32 v45, v45\n\tv_exp_f32 v46, v46\n\tv_exp_f32 v47, v47\n\tv_smfmac_f32_32x32x32_bf16 v[16:31], v[116:119], v[80:87], v140\n\t" \
  "ds_read_b128 v[116:119], v143 offset:1536\n\tv_exp_f32 v48, v48\n\tv_exp_f32 v49, v49\n\tv_exp_f32 v50, v50\n\t" \
  "v_exp_f32 v51, v51\n\tv_mfma_f32_32x32x8_bf16 v[64:79], v[132:133], v[106:107], v[64:79]\n\tv_exp_f32 v52, v52\n\tv_exp_f32 v53, v53\n\t" \
  "v_exp_f32 v54, v54\n\tv_exp_f32 v55, v55\n\tv_smfmac_f32_32x32x32_bf16 v[16:31], v[120:123], v[88:95], v140\n\tds_read_b128 v[120:123], v143 offset:2304\n\t" \
  "v_exp_f32 v56, v56\n\tv_exp_f32 v57, v57\n\tv_exp_f32 v58, v58\n\tv_exp_f32 v59, v59\n\t" \
  "v_mfma_f32_32x32x16_bf16 v[80:95], v[128:131], v[100:103], 0\n\tds_read_b128 v[128:131], v141 offset:1024\n\tv_exp_f32 v60, v60\n\tv_exp_f32 v61, v61\n\t" \
  "v_exp_f32 v62, v62\n\tv_exp_f32 v63, v63\n\ts_waitcnt lgkmcnt(2)\n\tv_smfmac_f32_32x32x32_bf16 v[0:15], v[108:111], v[32:39], v140\n\t" \
  "v_exp_f32 v64, v64\n\tv_exp_f32 v65, v65\n\tv_exp_f32 v66, v66\n\tv_exp_f32 v67, v67\n\t" \
  "v_mfma_f32_32x32x8_bf16 v[80:95], v[134:135], v[106:107], v[80:95]\n\tv_exp_f32 v68, v68\n\tv_exp_f32 v69, v69\n\tv_exp_f32 v70, v70\n\t" \
  "v_exp_f32 v71, v71\n\tv_smfmac_f32_32x32x32_bf16 v[0:15], v[112:115], v[40:47], v140\n\tv_exp_f32 v72, v72\n\tv_exp_f32 v73, v73\n\t" \
  "v_exp_f32 v74, v74\n\tv_exp_f32 v75, v75\n\tv_mfma_f32_32x32x16_bf16 v[32:47], v[124:127], v[96:99], 0\n\tv_exp_f32 v76, v76\n\t" \
  "v_exp_f32 v77, v77\n\tv_exp_f32 v78, v78\n\tv_exp_f32 v79, v79\n\tv_smfmac_f32_32x32x32_bf16 v[0:15], v[116:119], v[48:55], v140\n\t" \
  "v_exp_f32 v80, v80\n\tv_exp_f32 v81, v81\n\tv_exp_f32 v82, v82\n\tv_exp_f32 v83, v83\n\t" \
  "v_mfma_f32_32x32x8_bf16 v[32:47], v[136:137], v[104:105], v[32:47]\n\tv_exp_f32 v84, v84\n\tv_exp_f32 v85, v85\n\tv_exp_f32 v86, v86\n\t" \
  "v_exp_f32 v87, v87\n\ts_waitcnt lgkmcnt(0)\n\tv_smfmac_f32_32x32x32_bf16 v[0:15], v[120:123], v[56:63], v140\n\tv_exp_f32 v88, v88\n\t" \
  "v_exp_f32 v89, v89\n\tv_exp_f32 v90, v90\n\tv_exp_f32 v91, v91\n\tv_mfma_f32_32x32x16_bf16 v[48:63], v[128:131], v[96:99], 0\n\t" \
  "v_exp_f32 v92, v92\n\tv_exp_f32 v93, v93\n\tv_exp_f32 v94, v94\n\tv_exp_f32 v95, v95\n\t" \
  "s_cmp_lg_u32 %[c], 2\n\ts_cbranch_scc1 4f\n\ts_barrier\n\t4:\n\t" \
  "v_smfmac_f32_32x32x32_bf16 v[16:31], v[108:111], v[64:71], v140\n\tds_read_b128 v[132:135], v142 offset:5632\n\tds_read_b128 v[108:111], v143 offset:5632\n\tv_exp_f32 v32, v32\n\t" \
  "v_exp_f32 v33, v33\n\tv_exp_f32 v34, v34\n\tv_exp_f32 v35, v35\n\tv_mfma_f32_32x32x8_bf16 v[48:63], v[138:139], v[104:105], v[48:63]\n\t" \
  "v_exp_f32 v36, v36\n\tv_exp_f32 v37, v37\n\tv_exp_f32 v38, v38\n\tv_exp_f32 v39, v39\n\t" \
  "v_smfmac_f32_32x32x32_bf16 v[16:31], v[112:115], v[72:79], v140\n\tds_read_b128 v[112:115], v143 offset:6400\n\tv_exp_f32 v40, v40\n\tv_exp_f32 v41, v41\n\t" \
  "v_exp_f32 v42, v42\n\tv_exp_f32 v43, v43\n\tv_mfma_f32_32x32x16_bf16 v[64:79], v[124:127], v[100:103], 0\n\tds_read_b128 v[124:127], v141 offset:5632\n\t" \
  "v_exp_f32 v44, v44\n\tv_exp_f32 v45, v45\n\tv_exp_f32 v46, v46\n\tv_exp_f32 v47, v47\n\t" \
  "v_smfmac_f32_32x32x32_bf16 v[16:31], v[116:119], v[80:87], v140\n\tds_read_b128 v[116:119], v143 offset:7168\n\tv_exp_f32 v48, v48\n\tv_exp_f32 v49, v49\n\t" \
  "v_exp_f32 v50, v50\n\tv_exp_f32 v51, v51\n\tv_mfma_f32_32x32x8_bf16 v[64:79], v[136:137], v[106:107], v[64:79]\n\tv_exp_f32 v52, v52\n\t" \
  "v_exp_f32 v53, v53\n\tv_exp_f32 v54, v54\n\tv_exp_f32 v55, v55\n\tv_smfmac_f32_32x32x32_bf16 v[16:31], v[120:123], v[88:95], v140\n\t" \
  "ds_read_b128 v[120:123], v143 offset:7936\n\tv_exp_f32 v56, v56\n\tv_exp_f32 v57, v57\n\tv_exp_f32 v58, v58\n\t" \
  "v_exp_f32 v59, v59\n\tv_mfma_f32_32x32x16_bf16 v[80:95], v[128:131], v[100:103], 0\n\tds_read_b128 v[128:131], v141 offset:6656\n\tv_exp_f32 v60, v60\n\t" \
  "v_exp_f32 v61, v61\n\tv_exp_f32 v62, v62\n\tv_exp_f32 v63, v63\n\ts_waitcnt lgkmcnt(2)\n\t" \
  "v_smfmac_f32_32x32x32_bf16 v[0:15], v[108:111], v[32:39], v140\n\tv_exp_f32 v64, v64\n\tv_exp_f32 v65, v65\n\tv_exp_f32 v66, v66\n\t" \
  "v_exp_f32 v67, v67\n\tv_mfma_f32_32x32x8_bf16 v[80:95], v[138:139], v[106:107], v[80:95]\n\tv_exp_f32 v68, v68\n\tv_exp_f32 v69, v69\n\t" \
  "v_exp_f32 v70, v70\n\tv_exp_f32 v71, v71\n\tv_smfmac_f32_32x32x32_bf16 v[0:15], v[112:115], v[40:47], v140\n\tv_exp_f32 v72, v72\n\t" \
  "v_exp_f32 v73, v73\n\tv_exp_f32 v74, v74\n\tv_exp_f32 v75, v75\n\tv_mfma_f32_32x32x16_bf16 v[32:47], v[124:127], v[96:99], 0\n\t" \
  "v_exp_f32 v76, v76\n\tv_exp_f32 v77, v77\n\tv_exp_f32 v78, v78\n\tv_exp_f32 v79, v79\n\t" \
  "v_smfmac_f32_32x32x32_bf16 v[0:15], v[116:119], v[48:55], v140\n\tv_exp_f32 v80, v80\n\tv_exp_f32 v81, v81\n\tv_exp_f32 v82, v82\n\t" \
  "v_exp_f32 v83, v83\n\tv_mfma_f32_32x32x8_bf16 v[32:47], v[132:133], v[104:105], v[32:47]\n\tv_exp_f32 v84, v84\n\tv_exp_f32 v85, v85\n\t" \
  "v_exp_f32 v86, v86\n\tv_exp_f32 v87, v87\n\ts_waitcnt lgkmcnt(0)\n\tv_smfmac_f32_32x32x32_bf16 v[0:15], v[120:123], v[56:63], v140\n\t" \
  "v_exp_f32 v88, v88\n\tv_exp_f32 v89, v89\n\tv_exp_f32 v90, v90\n\tv_exp_f32 v91, v91\n\t" \
  "v_mfma_f32_32x32x16_bf16 v[48:63], v[128:131], v[96:99], 0\n\tv_exp_f32 v92, v92\n\tv_exp_f32 v93, v93\n\tv_exp_f32 v94, v94\n\t" \
  "v_exp_f32 v95, v95\n\tv_smfmac_f32_32x32x32_bf16 v[16:31], v[108:111], v[64:71], v140\n\tds_read_b128 v[136:139], v142 offset:11264\n\tds_read_b128 v[108:111], v143 offset:11264\n\t" \
  "v_exp_f32 v32, v32\n\tv_exp_f32 v33, v33\n\tv_exp_f32 v34, v34\n\tv_exp_f32 v35, v35\n\t" \
  "v_mfma_f32_32x32x8_bf16 v[48:63], v[134:135], v[104:105], v[48:63]\n\tv_exp_f32 v36, v36\n\tv_exp_f32 v37, v37\n\tv_exp_f32 v38, v38\n\t" \
  "v_exp_f32 v39, v39\n\tv_smfmac_f32_32x32x32_bf16 v[16:31], v[112:115], v[72:79], v140\n\tds_read_b128 v[112:115], v143 offset:12032\n\tv_exp_f32 v40, v40\n\t" \
  "v_exp_f32 v41, v41\n\tv_exp_f32 v42, v42\n\tv_exp_f32 v43, v43\n\tv_mfma_f32_32x32x16_bf16 v[64:79], v[124:127], v[100:103], 0\n\t" \
  "ds_read_b128 v[124:127], v141 offset:11264\n\tv_exp_f32 v44, v44\n\tv_exp_f32 v45, v45\n\tv_exp_f32 v46, v46\n\t" \
  "v_exp_f32 v47, v47\n\tv_smfmac_f32_32x32x32_bf16 v[16:31], v[116:119], v[80:87], v140\n\tds_read_b128 v[116:119], v143 offset:12800\n\tv_exp_f32 v48, v48\n\t" \
  "v_exp_f32 v49, v49\n\tv_exp_f32 v50, v50\n\tv_exp_f32 v51, v51\n\tv_mfma_f32_32x32x8_bf16 v[64:79], v[132:133], v[106:107], v[64:79]\n\t" \
  "v_exp_f32 v52, v52\n\tv_exp_f32 v53, v53\n\tv_exp_f32 v54, v54\n\tv_exp_f32 v55, v55\n\t" \
  "v_smfmac_f32_32x32x32_bf16 v[16:31], v[120:123], v[88:95], v140\n\tds_read_b128 v[120:123], v143 offset:13568\n\tv_exp_f32 v56, v56\n\tv_exp_f32 v57, v57\n\t" \
  "v_exp_f32 v58, v58\n\tv_exp_f32 v59, v59\n\tv_mfma_f32_32x32x16_bf16 v[80:95], v[128:131], v[100:103], 0\n\tds_read_b128 v[128:131], v141 offset:12288\n\t" \
  "v_exp_f32 v60, v60\n\tv_exp_f32 v61, v61\n\tv_exp_f32 v62, v62\n\tv_exp_f32 v63, v63\n\t" \
  "s_waitcnt lgkmcnt(2)\n\tv_smfmac_f32_32x32x32_bf16 v[0:15], v[108:111], v[32:39], v140\n\tv_exp_f32 v64, v64\n\tv_exp_f32 v65, v65\n\t" \
  "v_exp_f32 v66, v66\n\tv_exp_f32 v67, v67\n\tv_mfma_f32_32x32x8_bf16 v[80:95], v[134:135], v[106:107], v[80:95]\n\tv_exp_f32 v68, v68\n\t" \
  "v_exp_f32 v69, v69\n\tv_exp_f32 v70, v70\n\tv_exp_f32 v71, v71\n\tv_smfmac_f32_32x32x32_bf16 v[0:15], v[112:115], v[40:47], v140\n\t" \
  "v_exp_f32 v72, v72\n\tv_exp_f32 v73, v73\n\tv_exp_f32 v74, v74\n\tv_exp_f32 v75, v75\n\t" \
  "v_mfma_f32_32x32x16_bf16 v[32:47], v[124:127], v[96:99], 0\n\tv_exp_f32 v76, v76\n\tv_exp_f32 v77, v77\n\tv_exp_f32 v78, v78\n\t" \
  "v_exp_f32 v79, v79\n\tv_smfmac_f32_32x32x32_bf16 v[0:15], v[116:119], v[48:55], v140\n\tv_exp_f32 v80, v80\n\tv_exp_f32 v81, v81\n\t" \
  "v_exp_f32 v82, v82\n\tv_exp_f32 v83, v83\n\tv_mfma_f32_32x32x8_bf16 v[32:47], v[136:137], v[104:105], v[32:47]\n\tv_exp_f32 v84, v84\n\t" \
  "v_exp_f32 v85, v85\n\tv_exp_f32 v86, v86\n\tv_exp_f32 v87, v87\n\ts_waitcnt lgkmcnt(0)\n\t" \
  "v_smfmac_f32_32x32x32_bf16 v[0:15], v[120:123], v[56:63], v140\n\tv_exp_f32 v88, v88\n\tv_exp_f32 v89, v89\n\tv_exp_f32 v90, v90\n\t" \
  "v_exp_f32 v91, v91\n\tv_mfma_f32_32x32x16_bf16 v[48:63], v[128:131], v[96:99], 0\n\tv_exp_f32 v92, v92\n\tv_exp_f32 v93, v93\n\t" \
  "v_exp_f32 v94, v94\n\tv_exp_f32 v95, v95\n\tv_smfmac_f32_32x32x32_bf16 v[16:31], v[108:111], v[64:71], v140\n\tds_read_b128 v[132:135], v142 offset:16896\n\t" \
  "ds_read_b128 v[108:111], v143 offset:16896\n\tv_exp_f32 v32, v32\n\tv_exp_f32 v33, v33\n\tv_exp_f32 v34, v34\n\t" \
  "v_exp_f32 v35, v35\n\tv_mfma_f32_32x32x8_bf16 v[48:63], v[138:139], v[104:105], v[48:63]\n\tv_exp_f32 v36, v36\n\tv_exp_f32 v37, v37\n\t" \
  "v_exp_f32 v38, v38\n\tv_exp_f32 v39, v39\n\tv_smfmac_f32_32x32x32_bf16 v[16:31], v[112:115], v[72:79], v140\n\tds_read_b128 v[112:115], v143 offset:17664\n\t" \
  "v_exp_f32 v40, v40\n\tv_exp_f32 v41, v41\n\tv_exp_f32 v42, v42\n\tv_exp_f32 v43, v43\n\t" \
  "v_mfma_f32_32x32x16_bf16 v[64:79], v[124:127], v[100:103], 0\n\tds_read_b128 v[124:127], v141 offset:16896\n\tv_exp_f32 v44, v44\n\tv_exp_f32 v45, v45\n\t" \
  "v_exp_f32 v46, v46\n\tv_exp_f32 v47, v47\n\tv_smfmac_f32_32x32x32_bf16 v[16:31], v[116:119], v[80:87], v140\n\tds_read_b128 v[116:119], v143 offset:18432\n\t" \
  "v_exp_f32 v48, v48\n\tv_exp_f32 v49, v49\n\tv_exp_f32 v50, v50\n\tv_exp_f32 v51, v51\n\t" \
  "v_mfma_f32_32x32x8_bf16 v[64:79], v[136:137], v[106:107], v[64:79]\n\tv_exp_f32 v52, v52\n\tv_exp_f32 v53, v53\n\tv_exp_f32 v54, v54\n\t" \
  "v_exp_f32 v55, v55\n\tv_smfmac_f32_32x32x32_bf16 v[16:31], v[120:123], v[88:95], v140\n\tds_read_b128 v[120:123], v143 offset:19200\n\tv_exp_f32 v56, v56\n\t" \
  "v_exp_f32 v57, v57\n\tv_exp_f32 v58, v58\n\tv_exp_f32 v59, v59\n\tv_mfma_f32_32x32x16_bf16 v[80:95], v[128:131], v[100:103], 0\n\t" \
  "ds_read_b128 v[128:131], v141 offset:17920\n\tv_exp_f32 v60, v60\n\tv_exp_f32 v61, v61\n\tv_exp_f32 v62, v62\n\t" \
  "v_exp_f32 v63, v63\n\ts_waitcnt lgkmcnt(2)\n\tv_smfmac_f32_32x32x32_bf16 v[0:15], v[108:111], v[32:39], v140\n\tv_exp_f32 v64, v64\n\t" \
  "v_exp_f32 v65, v65\n\tv_exp_f32 v66, v66\n\tv_exp_f32 v67, v67\n\tv_mfma_f32_32x32x8_bf16 v[80:95], v[138:139], v[106:107], v[80:95]\n\t" \
  "v_exp_f32 v68, v68\n\tv_exp_f32 v69, v69\n\tv_exp_f32 v70, v70\n\tv_exp_f32 v71, v71\n\t" \
  "v_smfmac_f32_32x32x32_bf16 v[0:15], v[112:115], v[40:47], v140\n\tv_exp_f32 v72, v72\n\tv_exp_f32 v73, v73\n\tv_exp_f32 v74, v74\n\t" \
  "v_exp_f32 v75, v75\n\tv_mfma_f32_32x32x16_bf16 v[32:47], v[124:127], v[96:99], 0\n\tv_exp_f32 v76, v76\n\tv_exp_f32 v77, v77\n\t" \
  "v_exp_f32 v78, v78\n\tv_exp_f32 v79, v79\n\tv_smfmac_f32_32x32x32_bf16 v[0:15], v[116:119], v[48:55], v140\n\tv_exp_f32 v80, v80\n\t" \
  "v_exp_f32 v81, v81\n\tv_exp_f32 v82, v82\n\tv_exp_f32 v83, v83\n\tv_mfma_f32_32x32x8_bf16 v[32:47], v[132:133], v[104:105], v[32:47]\n\t" \
  "v_exp_f32 v84, v84\n\tv_exp_f32 v85, v85\n\tv_exp_f32 v86, v86\n\tv_exp_f32 v87, v87\n\t" \
  "s_waitcnt lgkmcnt(0)\n\tv_smfmac_f32_32x32x32_bf16 v[0:15], v[120:123], v[56:63], v140\n\tv_exp_f32 v88, v88\n\tv_exp_f32 v89, v89\n\t" \
  "v_exp_f32 v90, v90\n\tv_exp_f32 v91, v91\n\tv_mfma_f32_32x32x16_bf16 v[48:63], v[128:131], v[96:99], 0\n\tv_exp_f32 v92, v92\n\t" \
  "v_exp_f32 v93, v93\n\tv_exp_f32 v94, v94\n\tv_exp_f32 v95, v95\n\tv_add_u32 v141, %[d], v141\n\t" \
  "v_add_u32 v142, %[d], v142\n\tv_add_u32 v143, %[d], v143\n\ts_sub_i32 %[d], 0, %[d]\n\ts_sub_u32 %[c], %[c], 1\n\t" \
  "s_and_b32 %[e], %[c], 15\n\ts_cbranch_scc1 1b\n\ts_cmp_lg_u32 %[c], 0\n\ts_cbranch_scc1 2f\n\t" \
  "s_barrier\n\tv_smfmac_f32_32x32x32_bf16 v[16:31], v[108:111], v[64:71], v140\n\tds_read_b128 v[136:139], v142 offset:0\n\tds_read_b128 v[108:111], v143 offset:0\n\t" \
  "v_exp_f32 v32, v32\n\tv_exp_f32 v33, v33\n\tv_exp_f32 v34, v34\n\tv_exp_f32 v35, v35\n\t" \
  "v_mfma_f32_32x32x8_bf16 v[48:63], v[134:135], v[104:105], v[48:63]\n\tv_exp_f32 v36, v36\n\tv_exp_f32 v37, v37\n\tv_exp_f32 v38, v38\n\t" \
  "v_exp_f32 v39, v39\n\tv_smfmac_f32_32x32x32_bf16 v[16:31], v[112:115], v[72:79], v140\n\tds_read_b128 v[112:115], v143 offset:768\n\tv_exp_f32 v40, v40\n\t" \
  "v_exp_f32 v41, v41\n\tv_exp_f32 v42, v42\n\tv_exp_f32 v43, v43\n\tv_mfma_f32_32x32x16_bf16 v[64:79], v[124:127], v[100:103], 0\n\t" \
  "ds_read_b128 v[124:127], v141 offset:0\n\tv_exp_f32 v44, v44\n\tv_exp_f32 v45, v45\n\tv_exp_f32 v46, v46\n\t" \
  "v_exp_f32 v47, v47\n\tv_smfmac_f32_32x32x32_bf16 v[16:31], v[116:119], v[80:87], v140\n\tds_read_b128 v[116:119], v143 offset:1536\n\tv_exp_f32 v48, v48\n\t" \
  "v_exp_f32 v49, v49\n\tv_exp_f32 v50, v50\n\tv_exp_f32 v51, v51\n\tv_mfma_f32_32x32x8_bf16 v[64:79], v[132:133], v[106:107], v[64:79]\n\t" \
  "v_exp_f32 v52, v52\n\tv_exp_f32 v53, v53\n\tv_exp_f32 v54, v54\n\tv_exp_f32 v55, v55\n\t" \
  "v_smfmac_f32_32x32x32_bf16 v[16:31], v[120:123], v[88:95], v140\n\tds_read_b128 v[120:123], v143 offset:2304\n\tv_exp_f32 v56, v56\n\tv_exp_f32 v57, v57\n\t" \
  "v_exp_f32 v58, v58\n\tv_exp_f32 v59, v59\n\tv_mfma_f32_32x32x16_bf16 v[80:95], v[128:131], v[100:103], 0\n\tds_read_b128 v[128:131], v141 offset:1024\n\t" \
  "v_exp_f32 v60, v60\n\tv_exp_f32 v61, v61\n\tv_exp_f32 v62, v62\n\tv_exp_f32 v63, v63\n\t" \
  "s_waitcnt lgkmcnt(2)\n\tv_smfmac_f32_32x32x32_bf16 v[0:15], v[108:111], v[32:39], v140\n\tv_exp_f32 v64, v64\n\tv_exp_f32 v65, v65\n\t" \
  "v_exp_f32 v66, v66\n\tv_exp_f32 v67, v67\n\tv_mfma_f32_32x32x8_bf16 v[80:95], v[134:135], v[106:107], v[80:95]\n\tv_exp_f32 v68, v68\n\t" \
  "v_exp_f32 v69, v69\n\tv_exp_f32 v70, v70\n\tv_exp_f32 v71, v71\n\tv_smfmac_f32_32x32x32_bf16 v[0:15], v[112:115], v[40:47], v140\n\t" \
  "v_exp_f32 v72, v72\n\tv_exp_f32 v73, v73\n\tv_exp_f32 v74, v74\n\tv_exp_f32 v75, v75\n\t" \
  "v_mfma_f32_32x32x16_bf16 v[32:47], v[124:127], v[96:99], 0\n\tv_exp_f32 v76, v76\n\tv_exp_f32 v77, v77\n\tv_exp_f32 v78, v78\n\t" \
  "v_exp_f32 v79, v79\n\tv_smfmac_f32_32x32x32_bf16 v[0:15], v[116:119], v[48:55], v140\n\tv_exp_f32 v80, v80\n\tv_exp_f32 v81, v81\n\t" \
  "v_exp_f32 v82, v82\n\tv_exp_f32 v83, v83\n\tv_mfma_f32_32x32x8_bf16 v[32:47], v[136:137], v[104:105], v[32:47]\n\tv_exp_f32 v84, v84\n\t" \
  "v_exp_f32 v85, v85\n\tv_exp_f32 v86, v86\n\tv_exp_f32 v87, v87\n\ts_waitcnt lgkmcnt(0)\n\t" \
  "v_smfmac_f32_32x32x32_bf16 v[0:15], v[120:123], v[56:63], v140\n\tv_exp_f32 v88, v88\n\tv_exp_f32 v89, v89\n\tv_exp_f32 v90, v90\n\t" \
  "v_exp_f32 v91, v91\n\tv_mfma_f32_32x32x16_bf16 v[48:63], v[128:131], v[96:99], 0\n\tv_exp_f32 v92, v92\n\tv_exp_f32 v93, v93\n\t" \
  "v_exp_f32 v94, v94\n\tv_exp_f32 v95, v95\n\tv_smfmac_f32_32x32x32_bf16 v[16:31], v[108:111], v[64:71], v140\n\tds_read_b128 v[132:135], v142 offset:5632\n\t" \
  "ds_read_b128 v[108:111], v143 offset:5632\n\tv_exp_f32 v32, v32\n\tv_exp_f32 v33, v33\n\tv_exp_f32 v34, v34\n\t" \
  "v_exp_f32 v35, v35\n\tv_mfma_f32_32x32x8_bf16 v[48:63], v[138:139], v[104:105], v[48:63]\n\tv_exp_f32 v36, v36\n\tv_exp_f32 v37, v37\n\t" \
  "v_exp_f32 v38, v38\n\tv_exp_f32 v39, v39\n\tv_smfmac_f32_32x32x32_bf16 v[16:31], v[112:115], v[72:79], v140\n\tds_read_b128 v[112:115], v143 offset:6400\n\t" \
  "v_exp_f32 v40, v40\n\tv_exp_f32 v41, v41\n\tv_exp_f32 v42, v42\n\tv_exp_f32 v43, v43\n\t" \
  "v_mfma_f32_32x32x16_bf16 v[64:79], v[124:127], v[100:103], 0\n\tds_read_b128 v[124:127], v141 offset:5632\n\tv_exp_f32 v44, v44\n\tv_exp_f32 v45, v45\n\t" \
  "v_exp_f32 v46, v46\n\tv_exp_f32 v47, v47\n\tv_smfmac_f32_32x32x32_bf16 v[16:31], v[116:119], v[80:87], v140\n\tds_read_b128 v[116:119], v143 offset:7168\n\t" \
  "v_exp_f32 v48, v48\n\tv_exp_f32 v49, v49\n\tv_exp_f32 v50, v50\n\tv_exp_f32 v51, v51\n\t" \
  "v_mfma_f32_32x32x8_bf16 v[64:79], v[136:137], v[106:107], v[64:79]\n\tv_exp_f32 v52, v52\n\tv_exp_f32 v53, v53\n\tv_exp_f32 v54, v54\n\t" \
  "v_exp_f32 v55, v55\n\tv_smfmac_f32_32x32x32_bf16 v[16:31], v[120:123], v[88:95], v140\n\tds_read_b128 v[120:123], v143 offset:7936\n\tv_exp_f32 v56, v56\n\t" \
  "v_exp_f32 v57, v57\n\tv_exp_f32 v58, v58\n\tv_exp_f32 v59, v59\n\tv_mfma_f32_32x32x16_bf16 v[80:95], v[128:131], v[100:103], 0\n\t" \
  "ds_read_b128 v[128:131], v141 offset:6656\n\tv_exp_f32 v60, v60\n\tv_exp_f32 v61, v61\n\tv_exp_f32 v62, v62\n\t" \
  "v_exp_f32 v63, v63\n\ts_waitcnt lgkmcnt(2)\n\tv_smfmac_f32_32x32x32_bf16 v[0:15], v[108:111], v[32:39], v140\n\tv_exp_f32 v64, v64\n\t" \
  "v_exp_f32 v65, v65\n\tv_exp_f32 v66, v66\n\tv_exp_f32 v67, v67\n\tv_mfma_f32_32x32x8_bf16 v[80:95], v[138:139], v[106:107], v[80:95]\n\t" \
  "v_exp_f32 v68, v68\n\tv_exp_f32 v69, v69\n\tv_exp_f32 v70, v70\n\tv_exp_f32 v71, v71\n\t" \
  "v_smfmac_f32_32x32x32_bf16 v[0:15], v[112:115], v[40:47], v140\n\tv_exp_f32 v72, v72\n\tv_exp_f32 v73, v73\n\tv_exp_f32 v74, v74\n\t" \
  "v_exp_f32 v75, v75\n\tv_mfma_f32_32x32x16_bf16 v[32:47], v[124:127], v[96:99], 0\n\tv_exp_f32 v76, v76\n\tv_exp_f32 v77, v77\n\t" \
  "v_exp_f32 v78, v78\n\tv_exp_f32 v79, v79\n\tv_smfmac_f32_32x32x32_bf16 v[0:15], v[116:119], v[48:55], v140\n\tv_exp_f32 v80, v80\n\t" \
  "v_exp_f32 v81, v81\n\tv_exp_f32 v82, v82\n\tv_exp_f32 v83, v83\n\tv_mfma_f32_32x32x8_bf16 v[32:47], v[132:133], v[104:105], v[32:47]\n\t" \
  "v_exp_f32 v84, v84\n\tv_exp_f32 v85, v85\n\tv_exp_f32 v86, v86\n\tv_exp_f32 v87, v87\n\t" \
  "s_waitcnt lgkmcnt(0)\n\tv_smfmac_f32_32x32x32_bf16 v[0:15], v[120:123], v[56:63], v140\n\tv_exp_f32 v88, v88\n\tv_exp_f32 v89, v89\n\t" \
  "v_exp_f32 v90, v90\n\tv_exp_f32 v91, v91\n\tv_mfma_f32_32x32x16_bf16 v[48:63], v[128:131], v[96:99], 0\n\tv_exp_f32 v92, v92\n\t" \
  "v_exp_f32 v93, v93\n\tv_exp_f32 v94, v94\n\tv_exp_f32 v95, v95\n\tv_smfmac_f32_32x32x32_bf16 v[16:31], v[108:111], v[64:71], v140\n\t" \
  "ds_read_b128 v[136:139], v142 offset:11264\n\tds_read_b128 v[108:111], v143 offset:11264\n\tv_exp_f32 v32, v32\n\tv_exp_f32 v33, v33\n\t" \
  "v_exp_f32 v34, v34\n\tv_exp_f32 v35, v35\n\tv_mfma_f32_32x32x8_bf16 v[48:63], v[134:135], v[104:105], v[48:63]\n\tbuffer_load_dwordx4 v[144:147], v238, %[ru], %[s0] offen\n\t" \
  "v_exp_f32 v36, v36\n\tv_exp_f32 v37, v37\n\tv_exp_f32 v38, v38\n\tv_exp_f32 v39, v39\n\t" \
  "v_smfmac_f32_32x32x32_bf16 v[16:31], v[112:115], v[72:79], v140\n\tds_read_b128 v[112:115], v143 offset:12032\n\tv_exp_f32 v40, v40\n\tv_exp_f32 v41, v41\n\t" \
  "v_exp_f32 v42, v42\n\tv_exp_f32 v43, v43\n\tv_mfma_f32_32x32x16_bf16 v[64:79], v[124:127], v[100:103], 0\n\tds_read_b128 v[124:127], v141 offset:11264\n\t" \
  "v_exp_f32 v44, v44\n\tv_exp_f32 v45, v45\n\tv_exp_f32 v46, v46\n\tv_exp_f32 v47, v47\n\t" \
  "v_smfmac_f32_32x32x32_bf16 v[16:31], v[116:119], v[80:87], v140\n\tds_read_b128 v[116:119], v143 offset:12800\n\tv_exp_f32 v48, v48\n\tv_exp_f32 v49, v49\n\t" \
  "v_exp_f32 v50, v50\n\tv_exp_f32 v51, v51\n\tv_mfma_f32_32x32x8_bf16 v[64:79], v[132:133], v[106:107], v[64:79]\n\tbuffer_load_dwordx4 v[148:151], v239, %[ru], %[s0] offen\n\t" \
  "v_exp_f32 v52, v52\n\tv_exp_f32 v53, v53\n\tv_exp_f32 v54, v54\n\tv_exp_f32 v55, v55\n\t" \
  "v_smfmac_f32_32x32x32_bf16 v[16:31], v[120:123], v[88:95], v140\n\tds_read_b128 v[120:123], v143 offset:13568\n\tv_exp_f32 v56, v56\n\tv_exp_f32 v57, v57\n\t" \
  "v_exp_f32 v58, v58\n\tv_exp_f32 v59, v59\n\tv_mfma_f32_32x32x16_bf16 v[80:95], v[128:131], v[100:103], 0\n\tds_read_b128 v[128:131], v141 offset:12288\n\t" \
  "v_exp_f32 v60, v60\n\tv_exp_f32 v61, v61\n\tv_exp_f32 v62, v62\n\tv_exp_f32 v63, v63\n\t" \
  "s_waitcnt lgkmcnt(2)\n\tv_smfmac_f32_32x32x32_bf16 v[0:15], v[108:111], v[32:39], v140\n\tv_exp_f32 v64, v64\n\tv_exp_f32 v65, v65\n\t" \
  "v_exp_f32 v66, v66\n\tv_exp_f32 v67, v67\n\tv_mfma_f32_32x32x8_bf16 v[80:95], v[134:135], v[106:107], v[80:95]\n\tbuffer_load_dwordx4 v[152:155], v240, %[ru], %[s0] offen\n\t" \
  "v_exp_f32 v68, v68\n\tv_exp_f32 v69, v69\n\tv_exp_f32 v70, v70\n\tv_exp_f32 v71, v71\n\t" \
  "v_smfmac_f32_32x32x32_bf16 v[0:15], v[112:115], v[40:47], v140\n\tbuffer_load_dwordx4 v[156:159], v238, %[ru], %[s1] offen\n\tv_exp_f32 v72, v72\n\tv_exp_f32 v73, v73\n\t" \
  "v_exp_f32 v74, v74\n\tv_exp_f32 v75, v75\n\tv_mfma_f32_32x32x16_bf16 v[32:47], v[124:127], v[96:99], 0\n\tbuffer_load_dwordx4 v[160:163], v239, %[ru], %[s1] offen\n\t" \
  "v_exp_f32 v76, v76\n\tv_exp_f32 v77, v77\n\tv_exp_f32 v78, v78\n\tv_exp_f32 v79, v79\n\t" \
  "v_smfmac_f32_32x32x32_bf16 v[0:15], v[116:119], v[48:55], v140\n\tv_exp_f32 v80, v80\n\tv_exp_f32 v81, v81\n\tv_exp_f32 v82, v82\n\t" \
  "v_exp_f32 v83, v83\n\tv_mfma_f32_32x32x8_bf16 v[32:47], v[136:137], v[104:105], v[32:47]\n\tbuffer_load_dwordx4 v[164:167], v240, %[ru], %[s1] offen\n\tv_exp_f32 v84, v84\n\t" \
  "v_exp_f32 v85, v85\n\tv_exp_f32 v86, v86\n\tv_exp_f32 v87, v87\n\ts_waitcnt lgkmcnt(0)\n\t" \
  "v_smfmac_f32_32x32x32_bf16 v[0:15], v[120:123], v[56:63], v140\n\tv_exp_f32 v88, v88\n\tv_exp_f32 v89, v89\n\tv_exp_f32 v90, v90\n\t" \
  "v_exp_f32 v91, v91\n\tv_mfma_f32_32x32x16_bf16 v[48:63], v[128:131], v[96:99], 0\n\tbuffer_load_dwordx4 v[168:171], v238, %[ru], %[s2] offen\n\tv_exp_f32 v92, v92\n\t" \
  "v_exp_f32 v93, v93\n\tv_exp_f32 v94, v94\n\tv_exp_f32 v95, v95\n\tv_smfmac_f32_32x32x32_bf16 v[16:31], v[108:111], v[64:71], v140\n\t" \
  "ds_read_b128 v[132:135], v142 offset:16896\n\tds_read_b128 v[108:111], v143 offset:16896\n\tv_exp_f32 v32, v32\n\tv_exp_f32 v33, v33\n\t" \
  "v_exp_f32 v34, v34\n\tv_exp_f32 v35, v35\n\tv_mfma_f32_32x32x8_bf16 v[48:63], v[138:139], v[104:105], v[48:63]\n\tbuffer_load_dwordx4 v[172:175], v239, %[ru], %[s2] offen\n\t" \
  "v_exp_f32 v36, v36\n\tv_exp_f32 v37, v37\n\tv_exp_f32 v38, v38\n\tv_exp_f32 v39, v39\n\t" \
  "v_smfmac_f32_32x32x32_bf16 v[16:31], v[112:115], v[72:79], v140\n\tds_read_b128 v[112:115], v143 offset:17664\n\tv_exp_f32 v40, v40\n\tv_exp_f32 v41, v41\n\t" \
  "v_exp_f32 v42, v42\n\tv_exp_f32 v43, v43\n\tv_mfma_f32_32x32x16_bf16 v[64:79], v[124:127], v[100:103], 0\n\tds_read_b128 v[124:127], v141 offset:16896\n\t" \
  "v_exp_f32 v44, v44\n\tv_exp_f32 v45, v45\n\tv_exp_f32 v46, v46\n\tv_exp_f32 v47, v47\n\t" \
  "v_smfmac_f32_32x32x32_bf16 v[16:31], v[116:119], v[80:87], v140\n\tds_read_b128 v[116:119], v143 offset:18432\n\tv_exp_f32 v48, v48\n\tv_exp_f32 v49, v49\n\t" \
  "v_exp_f32 v50, v50\n\tv_exp_f32 v51, v51\n\tv_mfma_f32_32x32x8_bf16 v[64:79], v[136:137], v[106:107], v[64:79]\n\tbuffer_load_dwordx4 v[176:179], v240, %[ru], %[s2] offen\n\t" \
  "v_exp_f32 v52, v52\n\tv_exp_f32 v53, v53\n\tv_exp_f32 v54, v54\n\tv_exp_f32 v55, v55\n\t" \
  "v_smfmac_f32_32x32x32_bf16 v[16:31], v[120:123], v[88:95], v140\n\tds_read_b128 v[120:123], v143 offset:19200\n\tv_exp_f32 v56, v56\n\tv_exp_f32 v57, v57\n\t" \
  "v_exp_f32 v58, v58\n\tv_exp_f32 v59, v59\n\tv_mfma_f32_32x32x16_bf16 v[80:95], v[128:131], v[100:103], 0\n\tds_read_b128 v[128:131], v141 offset:17920\n\t" \
  "v_exp_f32 v60, v60\n\tv_exp_f32 v61, v61\n\tv_exp_f32 v62, v62\n\tv_exp_f32 v63, v63\n\t" \
  "s_waitcnt lgkmcnt(2)\n\tv_smfmac_f32_32x32x32_bf16 v[0:15], v[108:111], v[32:39], v140\n\tv_exp_f32 v64, v64\n\tv_exp_f32 v65, v65\n\t" \
  "v_exp_f32 v66, v66\n\tv_exp_f32 v67, v67\n\tv_mfma_f32_32x32x8_bf16 v[80:95], v[138:139], v[106:107], v[80:95]\n\tbuffer_load_dwordx4 v[180:183], v238, %[ru], %[s3] offen\n\t" \
  "v_exp_f32 v68, v68\n\tv_exp_f32 v69, v69\n\tv_exp_f32 v70, v70\n\tv_exp_f32 v71, v71\n\t" \
  "v_smfmac_f32_32x32x32_bf16 v[0:15], v[112:115], v[40:47], v140\n\tbuffer_load_dwordx4 v[184:187], v239, %[ru], %[s3] offen\n\tv_exp_f32 v72, v72\n\tv_exp_f32 v73, v73\n\t" \
  "v_exp_f32 v74, v74\n\tv_exp_f32 v75, v75\n\tv_mfma_f32_32x32x16_bf16 v[32:47], v[124:127], v[96:99], 0\n\tbuffer_load_dwordx4 v[188:191], v240, %[ru], %[s3] offen\n\t" \
  "v_exp_f32 v76, v76\n\tv_exp_f32 v77, v77\n\tv_exp_f32 v78, v78\n\tv_exp_f32 v79, v79\n\t" \
  "v_smfmac_f32_32x32x32_bf16 v[0:15], v[116:119], v[48:55], v140\n\tv_exp_f32 v80, v80\n\tv_exp_f32 v81, v81\n\tv_exp_f32 v82, v82\n\t" \
  "v_exp_f32 v83, v83\n\tv_mfma_f32_32x32x8_bf16 v[32:47], v[132:133], v[104:105], v[32:47]\n\tbuffer_load_dwordx4 v[192:195], v241, %[ru], %[s4] offen\n\tv_exp_f32 v84, v84\n\t" \
  "v_exp_f32 v85, v85\n\tv_exp_f32 v86, v86\n\tv_exp_f32 v87, v87\n\ts_waitcnt lgkmcnt(0)\n\t" \
  "v_smfmac_f32_32x32x32_bf16 v[0:15], v[120:123], v[56:63], v140\n\tv_exp_f32 v88, v88\n\tv_exp_f32 v89, v89\n\tv_exp_f32 v90, v90\n\t" \
  "v_exp_f32 v91, v91\n\tv_mfma_f32_32x32x16_bf16 v[48:63], v[128:131], v[96:99], 0\n\tv_exp_f32 v92, v92\n\tv_exp_f32 v93, v93\n\t" \
  "v_exp_f32 v94, v94\n\tv_exp_f32 v95, v95\n\tv_add_u32 v141, %[d], v141\n\tv_add_u32 v142, %[d], v142\n\t" \
  "v_add_u32 v143, %[d], v143\n\ts_sub_i32 %[d], 0, %[d]\n\ts_barrier\n\tv_smfmac_f32_32x32x32_bf16 v[16:31], v[108:111], v[64:71], v140\n\t" \
  "ds_read_b128 v[136:139], v142 offset:0\n\tds_read_b128 v[108:111], v143 offset:0\n\tv_exp_f32 v32, v32\n\tv_exp_f32 v33, v33\n\t" \
  "v_exp_f32 v34, v34\n\tv_exp_f32 v35, v35\n\tv_mfma_f32_32x32x8_bf16 v[48:63], v[134:135], v[104:105], v[48:63]\n\tv_exp_f32 v36, v36\n\t" \
  "v_exp_f32 v37, v37\n\tv_exp_f32 v38, v38\n\tv_exp_f32 v39, v39\n\tv_smfmac_f32_32x32x32_bf16 v[16:31], v[112:115], v[72:79], v140\n\t" \
  "ds_read_b128 v[112:115], v143 offset:768\n\tv_exp_f32 v40, v40\n\tv_exp_f32 v41, v41\n\tv_exp_f32 v42, v42\n\t" \
  "v_exp_f32 v43, v43\n\tv_mfma_f32_32x32x16_bf16 v[64:79], v[124:127], v[100:103], 0\n\tds_read_b128 v[124:127], v141 offset:0\n\tv_exp_f32 v44, v44\n\t" \
  "v_exp_f32 v45, v45\n\tv_exp_f32 v46, v46\n\tv_exp_f32 v47, v47\n\tv_smfmac_f32_32x32x32_bf16 v[16:31], v[116:119], v[80:87], v140\n\t" \
  "ds_read_b128 v[116:119], v143 offset:1536\n\tv_exp_f32 v48, v48\n\tv_exp_f32 v49, v49\n\tv_exp_f32 v50, v50\n\t" \
  "v_exp_f32 v51, v51\n\tv_mfma_f32_32x32x8_bf16 v[64:79], v[132:133], v[106:107], v[64:79]\n\tv_exp_f32 v52, v52\n\tv_exp_f32 v53, v53\n\t" \
  "v_exp_f32 v54, v54\n\tv_exp_f32 v55, v55\n\tv_smfmac_f32_32x32x32_bf16 v[16:31], v[120:123], v[88:95], v140\n\tds_read_b128 v[120:123], v143 offset:2304\n\t" \
  "v_exp_f32 v56, v56\n\tv_exp_f32 v57, v57\n\tv_exp_f32 v58, v58\n\tv_exp_f32 v59, v59\n\t" \
  "v_mfma_f32_32x32x16_bf16 v[80:95], v[128:131], v[100:103], 0\n\tds_read_b128 v[128:131], v141 offset:1024\n\tv_exp_f32 v60, v60\n\tv_exp_f32 v61, v61\n\t" \
  "v_exp_f32 v62, v62\n\tv_exp_f32 v63, v63\n\ts_waitcnt lgkmcnt(2)\n\tv_smfmac_f32_32x32x32_bf16 v[0:15], v[108:111], v[32:39], v140\n\t" \
  "v_exp_f32 v64, v64\n\tv_exp_f32 v65, v65\n\tv_exp_f32 v66, v66\n\tv_exp_f32 v67, v67\n\t" \
  "v_mfma_f32_32x32x8_bf16 v[80:95], v[134:135], v[106:107], v[80:95]\n\tv_exp_f32 v68, v68\n\tv_exp_f32 v69, v69\n\tv_exp_f32 v70, v70\n\t" \
  "v_exp_f32 v71, v71\n\tv_smfmac_f32_32x32x32_bf16 v[0:15], v[112:115], v[40:47], v140\n\tv_exp_f32 v72, v72\n\tv_exp_f32 v73, v73\n\t" \
  "v_exp_f32 v74, v74\n\tv_exp_f32 v75, v75\n\tv_mfma_f32_32x32x16_bf16 v[32:47], v[124:127], v[96:99], 0\n\tv_exp_f32 v76, v76\n\t" \
  "v_exp_f32 v77, v77\n\tv_exp_f32 v78, v78\n\tv_exp_f32 v79, v79\n\tv_smfmac_f32_32x32x32_bf16 v[0:15], v[116:119], v[48:55], v140\n\t" \
  "v_exp_f32 v80, v80\n\tv_exp_f32 v81, v81\n\tv_exp_f32 v82, v82\n\tv_exp_f32 v83, v83\n\t" \
  "v_mfma_f32_32x32x8_bf16 v[32:47], v[136:137], v[104:105], v[32:47]\n\tv_exp_f32 v84, v84\n\tv_exp_f32 v85, v85\n\tv_exp_f32 v86, v86\n\t" \
  "v_exp_f32 v87, v87\n\ts_waitcnt lgkmcnt(0)\n\tv_smfmac_f32_32x32x32_bf16 v[0:15], v[120:123], v[56:63], v140\n\tv_exp_f32 v88, v88\n\t" \
  "v_exp_f32 v89, v89\n\tv_exp_f32 v90, v90\n\tv_exp_f32 v91, v91\n\tv_mfma_f32_32x32x16_bf16 v[48:63], v[128:131], v[96:99], 0\n\t" \
  "v_exp_f32 v92, v92\n\tv_exp_f32 v93, v93\n\tv_exp_f32 v94, v94\n\tv_exp_f32 v95, v95\n\t" \
  "v_smfmac_f32_32x32x32_bf16 v[16:31], v[108:111], v[64:71], v140\n\tds_read_b128 v[132:135], v142 offset:5632\n\tds_read_b128 v[108:111], v143 offset:5632\n\tv_exp_f32 v32, v32\n\t" \
  "v_exp_f32 v33, v33\n\tv_exp_f32 v34, v34\n\tv_exp_f32 v35, v35\n\tv_mfma_f32_32x32x8_bf16 v[48:63], v[138:139], v[104:105], v[48:63]\n\t" \
  "v_exp_f32 v36, v36\n\tv_exp_f32 v37, v37\n\tv_exp_f32 v38, v38\n\tv_exp_f32 v39, v39\n\t" \
  "v_smfmac_f32_32x32x32_bf16 v[16:31], v[112:115], v[72:79], v140\n\tds_read_b128 v[112:115], v143 offset:6400\n\tv_exp_f32 v40, v40\n\tv_exp_f32 v41, v41\n\t" \
  "v_exp_f32 v42, v42\n\tv_exp_f32 v43, v43\n\tv_mfma_f32_32x32x16_bf16 v[64:79], v[124:127], v[100:103], 0\n\tds_read_b128 v[124:127], v141 offset:5632\n\t" \
  "v_exp_f32 v44, v44\n\tv_exp_f32 v45, v45\n\tv_exp_f32 v46, v46\n\tv_exp_f32 v47, v47\n\t" \
  "v_smfmac_f32_32x32x32_bf16 v[16:31], v[116:119], v[80:87], v140\n\tds_read_b128 v[116:119], v143 offset:7168\n\tv_exp_f32 v48, v48\n\tv_exp_f32 v49, v49\n\t" \
  "v_exp_f32 v50, v50\n\tv_exp_f32 v51, v51\n\tv_mfma_f32_32x32x8_bf16 v[64:79], v[136:137], v[106:107], v[64:79]\n\tv_exp_f32 v52, v52\n\t" \
  "v_exp_f32 v53, v53\n\tv_exp_f32 v54, v54\n\tv_exp_f32 v55, v55\n\tv_smfmac_f32_32x32x32_bf16 v[16:31], v[120:123], v[88:95], v140\n\t" \
  "ds_read_b128 v[120:123], v143 offset:7936\n\tv_exp_f32 v56, v56\n\tv_exp_f32 v57, v57\n\tv_exp_f32 v58, v58\n\t" \
  "v_exp_f32 v59, v59\n\tv_mfma_f32_32x32x16_bf16 v[80:95], v[128:131], v[100:103], 0\n\tds_read_b128 v[128:131], v141 offset:6656\n\tv_exp_f32 v60, v60\n\t" \
  "v_exp_f32 v61, v61\n\tv_exp_f32 v62, v62\n\tv_exp_f32 v63, v63\n\ts_waitcnt lgkmcnt(2)\n\t" \
  "v_smfmac_f32_32x32x32_bf16 v[0:15], v[108:111], v[32:39], v140\n\tv_exp_f32 v64, v64\n\tv_exp_f32 v65, v65\n\tv_exp_f32 v66, v66\n\t" \
  "v_exp_f32 v67, v67\n\tv_mfma_f32_32x32x8_bf16 v[80:95], v[138:139], v[106:107], v[80:95]\n\tv_exp_f32 v68, v68\n\tv_exp_f32 v69, v69\n\t" \
  "v_exp_f32 v70, v70\n\tv_exp_f32 v71, v71\n\tv_smfmac_f32_32x32x32_bf16 v[0:15], v[112:115], v[40:47], v140\n\tv_exp_f32 v72, v72\n\t" \
  "v_exp_f32 v73, v73\n\tv_exp_f32 v74, v74\n\tv_exp_f32 v75, v75\n\tv_mfma_f32_32x32x16_bf16 v[32:47], v[124:127], v[96:99], 0\n\t" \
  "v_exp_f32 v76, v76\n\tv_exp_f32 v77, v77\n\tv_exp_f32 v78, v78\n\tv_exp_f32 v79, v79\n\t" \
  "v_smfmac_f32_32x32x32_bf16 v[0:15], v[116:119], v[48:55], v140\n\tv_exp_f32 v80, v80\n\tv_exp_f32 v81, v81\n\tv_exp_f32 v82, v82\n\t" \
  "v_exp_f32 v83, v83\n\tv_mfma_f32_32x32x8_bf16 v[32:47], v[132:133], v[104:105], v[32:47]\n\tv_exp_f32 v84, v84\n\tv_exp_f32 v85, v85\n\t" \
  "v_exp_f32 v86, v86\n\tv_exp_f32 v87, v87\n\ts_waitcnt lgkmcnt(0)\n\tv_smfmac_f32_32x32x32_bf16 v[0:15], v[120:123], v[56:63], v140\n\t" \
  "v_exp_f32 v88, v88\n\tv_exp_f32 v89, v89\n\tv_exp_f32 v90, v90\n\tv_exp_f32 v91, v91\n\t" \
  "v_mfma_f32_32x32x16_bf16 v[48:63], v[128:131], v[96:99], 0\n\tv_exp_f32 v92, v92\n\tv_exp_f32 v93, v93\n\tv_exp_f32 v94, v94\n\t" \
  "v_exp_f32 v95, v95\n\tv_smfmac_f32_32x32x32_bf16 v[16:31], v[108:111], v[64:71], v140\n\tds_read_b128 v[136:139], v142 offset:11264\n\tds_read_b128 v[108:111], v143 offset:11264\n\t" \
  "v_exp_f32 v32, v32\n\tv_exp_f32 v33, v33\n\tv_exp_f32 v34, v34\n\tv_exp_f32 v35, v35\n\t" \
  "v_mfma_f32_32x32x8_bf16 v[48:63], v[134:135], v[104:105], v[48:63]\n\tbuffer_load_dwordx4 v[196:199], v242, %[ru], %[s4] offen\n\tv_exp_f32 v36, v36\n\tv_exp_f32 v37, v37\n\t" \
  "v_exp_f32 v38, v38\n\tv_exp_f32 v39, v39\n\tv_smfmac_f32_32x32x32_bf16 v[16:31], v[112:115], v[72:79], v140\n\tds_read_b128 v[112:115], v143 offset:12032\n\t" \
  "v_exp_f32 v40, v40\n\tv_exp_f32 v41, v41\n\tv_exp_f32 v42, v42\n\tv_exp_f32 v43, v43\n\t" \
  "v_mfma_f32_32x32x16_bf16 v[64:79], v[124:127], v[100:103], 0\n\tds_read_b128 v[124:127], v141 offset:11264\n\tv_exp_f32 v44, v44\n\tv_exp_f32 v45, v45\n\t" \
  "v_exp_f32 v46, v46\n\tv_exp_f32 v47, v47\n\tv_smfmac_f32_32x32x32_bf16 v[16:31], v[116:119], v[80:87], v140\n\tds_read_b128 v[116:119], v143 offset:12800\n\t" \
  "v_exp_f32 v48, v48\n\tv_exp_f32 v49, v49\n\tv_exp_f32 v50, v50\n\tv_exp_f32 v51, v51\n\t" \
  "v_mfma_f32_32x32x8_bf16 v[64:79], v[132:133], v[106:107], v[64:79]\n\tbuffer_load_dwordx4 v[200:203], v243, %[ru], %[s4] offen\n\tv_exp_f32 v52, v52\n\tv_exp_f32 v53, v53\n\t" \
  "v_exp_f32 v54, v54\n\tv_exp_f32 v55, v55\n\tv_smfmac_f32_32x32x32_bf16 v[16:31], v[120:123], v[88:95], v140\n\tds_read_b128 v[120:123], v143 offset:13568\n\t" \
  "v_exp_f32 v56, v56\n\tv_exp_f32 v57, v57\n\tv_exp_f32 v58, v58\n\tv_exp_f32 v59, v59\n\t" \
  "v_mfma_f32_32x32x16_bf16 v[80:95], v[128:131], v[100:103], 0\n\tds_read_b128 v[128:131], v141 offset:12288\n\tv_exp_f32 v60, v60\n\tv_exp_f32 v61, v61\n\t" \
  "v_exp_f32 v62, v62\n\tv_exp_f32 v63, v63\n\ts_waitcnt lgkmcnt(2)\n\tv_smfmac_f32_32x32x32_bf16 v[0:15], v[108:111], v[32:39], v140\n\t" \
  "v_exp_f32 v64, v64\n\tv_exp_f32 v65, v65\n\tv_exp_f32 v66, v66\n\tv_exp_f32 v67, v67\n\t" \
  "v_mfma_f32_32x32x8_bf16 v[80:95], v[134:135], v[106:107], v[80:95]\n\tbuffer_load_dwordx4 v[204:207], v244, %[rg], %[s5] offen\n\tv_exp_f32 v68, v68\n\tv_exp_f32 v69, v69\n\t" \
  "v_exp_f32 v70, v70\n\tv_exp_f32 v71, v71\n\tv_smfmac_f32_32x32x32_bf16 v[0:15], v[112:115], v[40:47], v140\n\tbuffer_load_dwordx4 v[208:211], v245, %[rg], %[s5] offen\n\t" \
  "v_exp_f32 v72, v72\n\tv_exp_f32 v73, v73\n\tv_exp_f32 v74, v74\n\tv_exp_f32 v75, v75\n\t" \
  "v_mfma_f32_32x32x16_bf16 v[32:47], v[124:127], v[96:99], 0\n\tbuffer_load_dwordx4 v[212:215], v246, %[rg], %[s5] offen\n\tv_exp_f32 v76, v76\n\tv_exp_f32 v77, v77\n\t" \
  "v_exp_f32 v78, v78\n\tv_exp_f32 v79, v79\n\tv_smfmac_f32_32x32x32_bf16 v[0:15], v[116:119], v[48:55], v140\n\tv_exp_f32 v80, v80\n\t" \
  "v_exp_f32 v81, v81\n\tv_exp_f32 v82, v82\n\tv_exp_f32 v83, v83\n\tv_mfma_f32_32x32x8_bf16 v[32:47], v[136:137], v[104:105], v[32:47]\n\t" \
  "buffer_load_dwordx4 v[216:219], v244, %[rg], %[s6] offen\n\tv_exp_f32 v84, v84\n\tv_exp_f32 v85, v85\n\tv_exp_f32 v86, v86\n\t" \
  "v_exp_f32 v87, v87\n\ts_waitcnt lgkmcnt(0)\n\tv_smfmac_f32_32x32x32_bf16 v[0:15], v[120:123], v[56:63], v140\n\tv_exp_f32 v88, v88\n\t" \
  "v_exp_f32 v89, v89\n\tv_exp_f32 v90, v90\n\tv_exp_f32 v91, v91\n\tv_mfma_f32_32x32x16_bf16 v[48:63], v[128:131], v[96:99], 0\n\t" \
  "buffer_load_dwordx4 v[220:223], v245, %[rg], %[s6] offen\n\tv_exp_f32 v92, v92\n\tv_exp_f32 v93, v93\n\tv_exp_f32 v94, v94\n\t" \
  "v_exp_f32 v95, v95\n\tv_smfmac_f32_32x32x32_bf16 v[16:31], v[108:111], v[64:71], v140\n\tds_read_b128 v[132:135], v142 offset:16896\n\tds_read_b128 v[108:111], v143 offset:16896\n\t" \
  "v_exp_f32 v32, v32\n\tv_exp_f32 v33, v33\n\tv_exp_f32 v34, v34\n\tv_exp_f32 v35, v35\n\t" \
  "v_mfma_f32_32x32x8_bf16 v[48:63], v[138:139], v[104:105], v[48:63]\n\tbuffer_load_dwordx4 v[224:227], v246, %[rg], %[s6] offen\n\tv_exp_f32 v36, v36\n\tv_exp_f32 v37, v37\n\t" \
  "v_exp_f32 v38, v38\n\tv_exp_f32 v39, v39\n\tv_smfmac_f32_32x32x32_bf16 v[16:31], v[112:115], v[72:79], v140\n\tds_read_b128 v[112:115], v143 offset:17664\n\t" \
  "v_exp_f32 v40, v40\n\tv_exp_f32 v41, v41\n\tv_exp_f32 v42, v42\n\tv_exp_f32 v43, v43\n\t" \
  "v_mfma_f32_32x32x16_bf16 v[64:79], v[124:127], v[100:103], 0\n\tds_read_b128 v[124:127], v141 offset:16896\n\tv_exp_f32 v44, v44\n\tv_exp_f32 v45, v45\n\t" \
  "v_exp_f32 v46, v46\n\tv_exp_f32 v47, v47\n\tv_smfmac_f32_32x32x32_bf16 v[16:31], v[116:119], v[80:87], v140\n\tds_read_b128 v[116:119], v143 offset:18432\n\t" \
  "v_exp_f32 v48, v48\n\tv_exp_f32 v49, v49\n\tv_exp_f32 v50, v50\n\tv_exp_f32 v51, v51\n\t" \
  "v_mfma_f32_32x32x8_bf16 v[64:79], v[136:137], v[106:107], v[64:79]\n\tbuffer_load_dwordx2 v[228:229], v247, %[rx], %[s7] offen offset:0\n\tv_exp_f32 v52, v52\n\tv_exp_f32 v53, v53\n\t" \
  "v_exp_f32 v54, v54\n\tv_exp_f32 v55, v55\n\tv_smfmac_f32_32x32x32_bf16 v[16:31], v[120:123], v[88:95], v140\n\tds_read_b128 v[120:123], v143 offset:19200\n\t" \
  "v_exp_f32 v56, v56\n\tv_exp_f32 v57, v57\n\tv_exp_f32 v58, v58\n\tv_exp_f32 v59, v59\n\t" \
  "v_mfma_f32_32x32x16_bf16 v[80:95], v[128:131], v[100:103], 0\n\tds_read_b128 v[128:131], v141 offset:17920\n\tv_exp_f32 v60, v60\n\tv_exp_f32 v61, v61\n\t" \
  "v_exp_f32 v62, v62\n\tv_exp_f32 v63, v63\n\ts_waitcnt lgkmcnt(2)\n\tv_smfmac_f32_32x32x32_bf16 v[0:15], v[108:111], v[32:39], v140\n\t" \
  "v_exp_f32 v64, v64\n\tv_exp_f32 v65, v65\n\tv_exp_f32 v66, v66\n\tv_exp_f32 v67, v67\n\t" \
  "v_mfma_f32_32x32x8_bf16 v[80:95], v[138:139], v[106:107], v[80:95]\n\tbuffer_load_dwordx2 v[230:231], v247, %[rx], %[s7] offen offset:32\n\tv_exp_f32 v68, v68\n\tv_exp_f32 v69, v69\n\t" \
  "v_exp_f32 v70, v70\n\tv_exp_f32 v71, v71\n\tv_smfmac_f32_32x32x32_bf16 v[0:15], v[112:115], v[40:47], v140\n\tbuffer_load_dwordx2 v[232:233], v247, %[rx], %[s7] offen offset:64\n\t" \
  "v_exp_f32 v72, v72\n\tv_exp_f32 v73, v73\n\tv_exp_f32 v74, v74\n\tv_exp_f32 v75, v75\n\t" \
  "v_mfma_f32_32x32x16_bf16 v[32:47], v[124:127], v[96:99], 0\n\tbuffer_load_dwordx2 v[234:235], v247, %[rx], %[s7] offen offset:96\n\tv_exp_f32 v76, v76\n\tv_exp_f32 v77, v77\n\t" \
  "v_exp_f32 v78, v78\n\tv_exp_f32 v79, v79\n\tv_smfmac_f32_32x32x32_bf16 v[0:15], v[116:119], v[48:55], v140\n\tv_exp_f32 v80, v80\n\t" \
  "v_exp_f32 v81, v81\n\tv_exp_f32 v82, v82\n\tv_exp_f32 v83, v83\n\tv_mfma_f32_32x32x8_bf16 v[32:47], v[132:133], v[104:105], v[32:47]\n\t" \
  "buffer_load_dwordx2 v[236:237], v247, %[rx], %[s7] offen offset:128\n\tv_exp_f32 v84, v84\n\tv_exp_f32 v85, v85\n\tv_exp_f32 v86, v86\n\t" \
  "v_exp_f32 v87, v87\n\ts_waitcnt lgkmcnt(0)\n\tv_smfmac_f32_32x32x32_bf16 v[0:15], v[120:123], v[56:63], v140\n\tv_exp_f32 v88, v88\n\t" \
  "v_exp_f32 v89, v89\n\tv_exp_f32 v90, v90\n\tv_exp_f32 v91, v91\n\tv_mfma_f32_32x32x16_bf16 v[48:63], v[128:131], v[96:99], 0\n\t" \
  "v_exp_f32 v92, v92\n\tv_exp_f32 v93, v93\n\tv_exp_f32 v94, v94\n\tv_exp_f32 v95, v95\n\t" \
  "v_add_u32 v141, %[d], v141\n\tv_add_u32 v142, %[d], v142\n\tv_add_u32 v143, %[d], v143\n\ts_sub_i32 %[d], 0, %[d]\n\t" \
  "s_mov_b32 %[c], 20\n\ts_branch 1b\n\t2:\n\tv_smfmac_f32_32x32x32_bf16 v[16:31], v[108:111], v[64:71], v140\n\t" \
  "v_smfmac_f32_32x32x32_bf16 v[16:31], v[112:115], v[72:79], v140\n\tv_smfmac_f32_32x32x32_bf16 v[16:31], v[116:119], v[80:87], v140\n\tv_smfmac_f32_32x32x32_bf16 v[16:31], v[120:123], v[88:95], v140\n\ts_waitcnt vmcnt(0)\n\t" \
  "s_nop 7\n\ts_nop 2\n\t" \
  ""
#define ZERO_ASM \
  "v_mov_b64 v[0:1], 0\n\tv_mov_b64 v[2:3], 0\n\tv_mov_b64 v[4:5], 0\n\tv_mov_b64 v[6:7], 0\n\t" \
  "v_mov_b64 v[8:9], 0\n\tv_mov_b64 v[10:11], 0\n\tv_mov_b64 v[12:13], 0\n\tv_mov_b64 v[14:15], 0\n\t" \
  "v_mov_b64 v[16:17], 0\n\tv_mov_b64 v[18:19], 0\n\tv_mov_b64 v[20:21], 0\n\tv_mov_b64 v[22:23], 0\n\t" \
  "v_mov_b64 v[24:25], 0\n\tv_mov_b64 v[26:27], 0\n\tv_mov_b64 v[28:29], 0\n\tv_mov_b64 v[30:31], 0\n\t" \
  "v_mov_b64 v[80:81], 0\n\tv_mov_b64 v[82:83], 0\n\tv_mov_b64 v[84:85], 0\n\tv_mov_b64 v[86:87], 0\n\t" \
  "v_mov_b64 v[88:89], 0\n\tv_mov_b64 v[90:91], 0\n\tv_mov_b64 v[92:93], 0\n\tv_mov_b64 v[94:95], 0\n\t" \
  "v_mov_b64 v[108:109], 0\n\tv_mov_b64 v[110:111], 0\n\tv_mov_b64 v[112:113], 0\n\tv_mov_b64 v[114:115], 0\n\t" \
  "v_mov_b64 v[116:117], 0\n\tv_mov_b64 v[118:119], 0\n\tv_mov_b64 v[120:121], 0\n\tv_mov_b64 v[122:123], 0\n\t" \
  ""
#define PA_ASM \
  "s_load_dwordx2 s[80:81], %[kp], 0x0\n\ts_load_dwordx2 s[84:85], %[kp], 0x8\n\ts_load_dwordx2 s[88:89], %[kp], 0x30\n\ts_load_dwordx2 s[92:93], %[kp], 0x10\n\t" \
  "s_load_dwordx16 s[64:79], %[kp], 0x0\n\tv_mbcnt_lo_u32_b32 v1, -1, 0\n\tv_mbcnt_hi_u32_b32 v1, -1, v1\n\ts_and_b32 s40, %[b], 7\n\t" \
  "s_lshr_b32 s41, %[b], 3\n\ts_lshr_b32 s45, %[b], 5\n\ts_lshr_b32 s42, s40, 1\n\ts_lshl_b32 s42, s42, 2\n\t" \
  "s_and_b32 s46, s41, 3\n\ts_add_u32 s42, s42, s46\n\ts_and_b32 s43, s40, 1\n\ts_lshl_b32 s43, s43, 3\n\t" \
  "s_add_u32 s43, s43, s45\n\ts_mov_b32 s82, 2621440\n\ts_mov_b32 s83, 0x20000\n\ts_mov_b32 s86, 2457600\n\t" \
  "s_mov_b32 s87, s83\n\ts_mov_b32 s90, 24117248\n\ts_mov_b32 s91, s83\n\ts_mov_b32 s94, 819200\n\t" \
  "s_mov_b32 s95, s83\n\ts_mov_b32 s59, 23265280\n\ts_mov_b32 s60, 23330816\n\ts_cmp_lt_u32 %[wv], 6\n\t" \
  "s_cbranch_scc1 2f\n\ts_sub_u32 s47, %[wv], 6\n\ts_lshl_b32 s46, s42, 4\n\ts_lshl_b32 s50, s45, 1\n\t" \
  "s_add_u32 s46, s46, s50\n\ts_add_u32 s46, s46, s47\n\ts_lshl_b32 s46, s46, 3\n\tv_lshrrev_b32 v2, 3, v1\n\t" \
  "v_add_u32 v2, s46, v2\n\tv_and_b32 v3, 7, v1\n\tv_mul_u32_u24 v10, 1280, v2\n\tv_lshl_add_u32 v10, v3, 4, v10\n\t" \
  "s_and_b32 s51, s40, 1\n\ts_lshl_b32 s51, s51, 11\n\ts_lshl_b32 s50, s41, 1\n\ts_add_u32 s50, s50, s47\n\t" \
  "s_mul_i32 s50, s50, 3840\n\tv_bfe_u32 v4, v1, 4, 1\n\tv_mul_u32_u24 v4, 245760, v4\n\tv_and_b32 v5, 15, v1\n\t" \
  "v_lshl_add_u32 v5, v5, 7, s51\n\tv_min_u32 v5, 3712, v5\n\tv_add3_u32 v11, v4, v5, s50\n\ts_waitcnt lgkmcnt(0)\n\t" \
  "buffer_load_dwordx4 v[24:27], v10, s[80:83], 0 offen offset:0\n\tbuffer_load_dwordx4 v[28:31], v10, s[80:83], 0 offen offset:128\n\tbuffer_load_dword v18, v11, s[84:87], 0 offen\n\tbuffer_load_dwordx4 v[32:35], v10, s[80:83], 0 offen offset:256\n\t" \
  "buffer_load_dwordx4 v[36:39], v10, s[80:83], 0 offen offset:384\n\ts_mov_b32 s46, 491520\n\tbuffer_load_dword v19, v11, s[84:87], s46 offen\n\tbuffer_load_dwordx2 v[16:17], off, s[88:91], s59 sc1\n\t" \
  "buffer_load_dwordx4 v[40:43], v10, s[80:83], 0 offen offset:512\n\tbuffer_load_dwordx4 v[44:47], v10, s[80:83], 0 offen offset:640\n\tbuffer_load_dwordx4 v[48:51], v10, s[80:83], 0 offen offset:768\n\tbuffer_load_dwordx4 v[52:55], v10, s[80:83], 0 offen offset:896\n\t" \
  "buffer_load_dwordx4 v[56:59], v10, s[80:83], 0 offen offset:1024\n\tbuffer_load_dwordx4 v[60:63], v10, s[80:83], 0 offen offset:1152\n\ts_cmp_eq_u32 s43, 15\n\ts_cbranch_scc1 101f\n\t" \
  "s_barrier\n\t101:\n\tv_add_u32 v2, s51, v2\n\tv_lshlrev_b32 v2, 4, v2\n\t" \
  "v_bfrev_b32 v12, 1\n\tv_cmp_eq_u32 vcc, 0, v3\n\ts_lshl_b32 s46, s42, 1\n\ts_add_u32 s46, s46, s47\n\t" \
  "s_lshl_b32 s46, s46, 6\n\ts_add_u32 s46, s46, s51\n\tv_cndmask_b32 v12, v12, v2, vcc\n\tv_add_lshl_u32 v13, v1, s46, 4\n\t" \
  "s_lshl_b32 s46, s47, 8\n\ts_add_u32 s46, s46, 131072\n\tv_lshl_add_u32 v14, v1, 2, s46\n\ts_mov_b32 s55, 0xffff0000\n\t" \
  "v_mov_b64 v[4:5], 0\n\tv_mov_b64 v[6:7], 0\n\tv_mov_b64 v[8:9], 0\n\ts_waitcnt vmcnt(10)\n\t" \
  "s_mov_b32 s46, 983040\n\tbuffer_load_dword v20, v11, s[84:87], s46 offen\n\ts_cmp_eq_u32 s43, 15\n\ts_cbranch_scc1 102f\n\t" \
  "s_barrier\n\t102:\n\ts_waitcnt vmcnt(13)\n\tv_lshlrev_b32 v64, 16, v24\n\t" \
  "v_and_b32 v65, s55, v24\n\tv_lshlrev_b32 v66, 16, v25\n\tv_and_b32 v67, s55, v25\n\tv_fmac_f32 v6, v64, v64\n\t" \
  "v_fmac_f32 v7, v65, v65\n\tv_fmac_f32 v8, v66, v66\n\tv_fmac_f32 v9, v67, v67\n\tv_add_f32 v64, v64, v65\n\t" \
  "v_add_f32 v66, v66, v67\n\tv_add_f32 v4, v4, v64\n\tv_add_f32 v5, v5, v66\n\tv_lshlrev_b32 v64, 16, v26\n\t" \
  "v_and_b32 v65, s55, v26\n\tv_lshlrev_b32 v66, 16, v27\n\tv_and_b32 v67, s55, v27\n\tv_fmac_f32 v6, v64, v64\n\t" \
  "v_fmac_f32 v7, v65, v65\n\tv_fmac_f32 v8, v66, v66\n\tv_fmac_f32 v9, v67, v67\n\tv_add_f32 v64, v64, v65\n\t" \
  "v_add_f32 v66, v66, v67\n\tv_add_f32 v4, v4, v64\n\tv_add_f32 v5, v5, v66\n\ts_waitcnt vmcnt(12)\n\t" \
  "v_lshlrev_b32 v64, 16, v28\n\tv_and_b32 v65, s55, v28\n\tv_lshlrev_b32 v66, 16, v29\n\tv_and_b32 v67, s55, v29\n\t" \
  "v_fmac_f32 v6, v64, v64\n\tv_fmac_f32 v7, v65, v65\n\tv_fmac_f32 v8, v66, v66\n\tv_fmac_f32 v9, v67, v67\n\t" \
  "v_add_f32 v64, v64, v65\n\tv_add_f32 v66, v66, v67\n\tv_add_f32 v4, v4, v64\n\tv_add_f32 v5, v5, v66\n\t" \
  "v_lshlrev_b32 v64, 16, v30\n\tv_and_b32 v65, s55, v30\n\tv_lshlrev_b32 v66, 16, v31\n\tv_and_b32 v67, s55, v31\n\t" \
  "v_fmac_f32 v6, v64, v64\n\tv_fmac_f32 v7, v65, v65\n\tv_fmac_f32 v8, v66, v66\n\tv_fmac_f32 v9, v67, v67\n\t" \
  "v_add_f32 v64, v64, v65\n\tv_add_f32 v66, v66, v67\n\tv_add_f32 v4, v4, v64\n\tv_add_f32 v5, v5, v66\n\t" \
  "s_waitcnt vmcnt(10)\n\tv_lshlrev_b32 v64, 16, v32\n\tv_and_b32 v65, s55, v32\n\tv_lshlrev_b32 v66, 16, v33\n\t" \
  "v_and_b32 v67, s55, v33\n\tv_fmac_f32 v6, v64, v64\n\tv_fmac_f32 v7, v65, v65\n\tv_fmac_f32 v8, v66, v66\n\t" \
  "v_fmac_f32 v9, v67, v67\n\tv_add_f32 v64, v64, v65\n\tv_add_f32 v66, v66, v67\n\tv_add_f32 v4, v4, v64\n\t" \
  "v_add_f32 v5, v5, v66\n\tv_lshlrev_b32 v64, 16, v34\n\tv_and_b32 v65, s55, v34\n\tv_lshlrev_b32 v66, 16, v35\n\t" \
  "v_and_b32 v67, s55, v35\n\tv_fmac_f32 v6, v64, v64\n\tv_fmac_f32 v7, v65, v65\n\tv_fmac_f32 v8, v66, v66\n\t" \
  "v_fmac_f32 v9, v67, v67\n\tv_add_f32 v64, v64, v65\n\tv_add_f32 v66, v66, v67\n\tv_add_f32 v4, v4, v64\n\t" \
  "v_add_f32 v5, v5, v66\n\ts_cmp_eq_u32 s43, 15\n\ts_cbranch_scc1 103f\n\ts_barrier\n\t" \
  "103:\n\ts_waitcnt vmcnt(9)\n\tv_lshlrev_b32 v64, 16, v36\n\tv_and_b32 v65, s55, v36\n\t" \
  "v_lshlrev_b32 v66, 16, v37\n\tv_and_b32 v67, s55, v37\n\tv_fmac_f32 v6, v64, v64\n\tv_fmac_f32 v7, v65, v65\n\t" \
  "v_fmac_f32 v8, v66, v66\n\tv_fmac_f32 v9, v67, v67\n\tv_add_f32 v64, v64, v65\n\tv_add_f32 v66, v66, v67\n\t" \
  "v_add_f32 v4, v4, v64\n\tv_add_f32 v5, v5, v66\n\tv_lshlrev_b32 v64, 16, v38\n\tv_and_b32 v65, s55, v38\n\t" \
  "v_lshlrev_b32 v66, 16, v39\n\tv_and_b32 v67, s55, v39\n\tv_fmac_f32 v6, v64, v64\n\tv_fmac_f32 v7, v65, v65\n\t" \
  "v_fmac_f32 v8, v66, v66\n\tv_fmac_f32 v9, v67, v67\n\tv_add_f32 v64, v64, v65\n\tv_add_f32 v66, v66, v67\n\t" \
  "v_add_f32 v4, v4, v64\n\tv_add_f32 v5, v5, v66\n\ts_mov_b32 s46, 1474560\n\tbuffer_load_dword v21, v11, s[84:87], s46 offen\n\t" \
  "s_waitcnt vmcnt(7)\n\tv_lshlrev_b32 v64, 16, v40\n\tv_and_b32 v65, s55, v40\n\tv_lshlrev_b32 v66, 16, v41\n\t" \
  "v_and_b32 v67, s55, v41\n\tv_fmac_f32 v6, v64, v64\n\tv_fmac_f32 v7, v65, v65\n\tv_fmac_f32 v8, v66, v66\n\t" \
  "v_fmac_f32 v9, v67, v67\n\tv_add_f32 v64, v64, v65\n\tv_add_f32 v66, v66, v67\n\tv_add_f32 v4, v4, v64\n\t" \
  "v_add_f32 v5, v5, v66\n\tv_lshlrev_b32 v64, 16, v42\n\tv_and_b32 v65, s55, v42\n\tv_lshlrev_b32 v66, 16, v43\n\t" \
  "v_and_b32 v67, s55, v43\n\tv_fmac_f32 v6, v64, v64\n\tv_fmac_f32 v7, v65, v65\n\tv_fmac_f32 v8, v66, v66\n\t" \
  "v_fmac_f32 v9, v67, v67\n\tv_add_f32 v64, v64, v65\n\tv_add_f32 v66, v66, v67\n\tv_add_f32 v4, v4, v64\n\t" \
  "v_add_f32 v5, v5, v66\n\ts_waitcnt vmcnt(6)\n\tv_lshlrev_b32 v64, 16, v44\n\tv_and_b32 v65, s55, v44\n\t" \
  "v_lshlrev_b32 v66, 16, v45\n\tv_and_b32 v67, s55, v45\n\tv_fmac_f32 v6, v64, v64\n\tv_fmac_f32 v7, v65, v65\n\t" \
  "v_fmac_f32 v8, v66, v66\n\tv_fmac_f32 v9, v67, v67\n\tv_add_f32 v64, v64, v65\n\tv_add_f32 v66, v66, v67\n\t" \
  "v_add_f32 v4, v4, v64\n\tv_add_f32 v5, v5, v66\n\tv_lshlrev_b32 v64, 16, v46\n\tv_and_b32 v65, s55, v46\n\t" \
  "v_lshlrev_b32 v66, 16, v47\n\tv_and_b32 v67, s55, v47\n\tv_fmac_f32 v6, v64, v64\n\tv_fmac_f32 v7, v65, v65\n\t" \
  "v_fmac_f32 v8, v66, v66\n\tv_fmac_f32 v9, v67, v67\n\tv_add_f32 v64, v64, v65\n\tv_add_f32 v66, v66, v67\n\t" \
  "v_add_f32 v4, v4, v64\n\tv_add_f32 v5, v5, v66\n\ts_cmp_eq_u32 s43, 15\n\ts_cbranch_scc1 104f\n\t" \
  "s_barrier\n\t104:\n\ts_waitcnt vmcnt(5)\n\tv_lshlrev_b32 v64, 16, v48\n\t" \
  "v_and_b32 v65, s55, v48\n\tv_lshlrev_b32 v66, 16, v49\n\tv_and_b32 v67, s55, v49\n\tv_fmac_f32 v6, v64, v64\n\t" \
  "v_fmac_f32 v7, v65, v65\n\tv_fmac_f32 v8, v66, v66\n\tv_fmac_f32 v9, v67, v67\n\tv_add_f32 v64, v64, v65\n\t" \
  "v_add_f32 v66, v66, v67\n\tv_add_f32 v4, v4, v64\n\tv_add_f32 v5, v5, v66\n\tv_lshlrev_b32 v64, 16, v50\n\t" \
  "v_and_b32 v65, s55, v50\n\tv_lshlrev_b32 v66, 16, v51\n\tv_and_b32 v67, s55, v51\n\tv_fmac_f32 v6, v64, v64\n\t" \
  "v_fmac_f32 v7, v65, v65\n\tv_fmac_f32 v8, v66, v66\n\tv_fmac_f32 v9, v67, v67\n\tv_add_f32 v64, v64, v65\n\t" \
  "v_add_f32 v66, v66, v67\n\tv_add_f32 v4, v4, v64\n\tv_add_f32 v5, v5, v66\n\ts_mov_b32 s46, 1966080\n\t" \
  "buffer_load_dword v22, v11, s[84:87], s46 offen\n\ts_waitcnt vmcnt(5)\n\tv_lshlrev_b32 v64, 16, v52\n\tv_and_b32 v65, s55, v52\n\t" \
  "v_lshlrev_b32 v66, 16, v53\n\tv_and_b32 v67, s55, v53\n\tv_fmac_f32 v6, v64, v64\n\tv_fmac_f32 v7, v65, v65\n\t" \
  "v_fmac_f32 v8, v66, v66\n\tv_fmac_f32 v9, v67, v67\n\tv_add_f32 v64, v64, v65\n\tv_add_f32 v66, v66, v67\n\t" \
  "v_add_f32 v4, v4, v64\n\tv_add_f32 v5, v5, v66\n\tv_lshlrev_b32 v64, 16, v54\n\tv_and_b32 v65, s55, v54\n\t" \
  "v_lshlrev_b32 v66, 16, v55\n\tv_and_b32 v67, s55, v55\n\tv_fmac_f32 v6, v64, v64\n\tv_fmac_f32 v7, v65, v65\n\t" \
  "v_fmac_f32 v8, v66, v66\n\tv_fmac_f32 v9, v67, v67\n\tv_add_f32 v64, v64, v65\n\tv_add_f32 v66, v66, v67\n\t" \
  "v_add_f32 v4, v4, v64\n\tv_add_f32 v5, v5, v66\n\ts_waitcnt vmcnt(4)\n\tv_lshlrev_b32 v64, 16, v56\n\t" \
  "v_and_b32 v65, s55, v56\n\tv_lshlrev_b32 v66, 16, v57\n\tv_and_b32 v67, s55, v57\n\tv_fmac_f32 v6, v64, v64\n\t" \
  "v_fmac_f32 v7, v65, v65\n\tv_fmac_f32 v8, v66, v66\n\tv_fmac_f32 v9, v67, v67\n\tv_add_f32 v64, v64, v65\n\t" \
  "v_add_f32 v66, v66, v67\n\tv_add_f32 v4, v4, v64\n\tv_add_f32 v5, v5, v66\n\tv_lshlrev_b32 v64, 16, v58\n\t" \
  "v_and_b32 v65, s55, v58\n\tv_lshlrev_b32 v66, 16, v59\n\tv_and_b32 v67, s55, v59\n\tv_fmac_f32 v6, v64, v64\n\t" \
  "v_fmac_f32 v7, v65, v65\n\tv_fmac_f32 v8, v66, v66\n\tv_fmac_f32 v9, v67, v67\n\tv_add_f32 v64, v64, v65\n\t" \
  "v_add_f32 v66, v66, v67\n\tv_add_f32 v4, v4, v64\n\tv_add_f32 v5, v5, v66\n\ts_cmp_eq_u32 s43, 15\n\t" \
  "s_cbranch_scc1 105f\n\ts_barrier\n\t105:\n\ts_waitcnt vmcnt(3)\n\t" \
  "v_lshlrev_b32 v64, 16, v60\n\tv_and_b32 v65, s55, v60\n\tv_lshlrev_b32 v66, 16, v61\n\tv_and_b32 v67, s55, v61\n\t" \
  "v_fmac_f32 v6, v64, v64\n\tv_fmac_f32 v7, v65, v65\n\tv_fmac_f32 v8, v66, v66\n\tv_fmac_f32 v9, v67, v67\n\t" \
  "v_add_f32 v64, v64, v65\n\tv_add_f32 v66, v66, v67\n\tv_add_f32 v4, v4, v64\n\tv_add_f32 v5, v5, v66\n\t" \
  "v_lshlrev_b32 v64, 16, v62\n\tv_and_b32 v65, s55, v62\n\tv_lshlrev_b32 v66, 16, v63\n\tv_and_b32 v67, s55, v63\n\t" \
  "v_fmac_f32 v6, v64, v64\n\tv_fmac_f32 v7, v65, v65\n\tv_fmac_f32 v8, v66, v66\n\tv_fmac_f32 v9, v67, v67\n\t" \
  "v_add_f32 v64, v64, v65\n\tv_add_f32 v66, v66, v67\n\tv_add_f32 v4, v4, v64\n\tv_add_f32 v5, v5, v66\n\t" \
  "s_waitcnt vmcnt(9)\n\tv_add_f32 v6, v6, v7\n\tv_add_f32 v8, v8, v9\n\tv_add_f32 v4, v4, v5\n\t" \
  "v_add_f32 v8, v6, v8\n\tv_readfirstlane_b32 s52, v16\n\ts_nop 0\n\tv_add_f32_dpp v4, v4, v4 quad_perm:[1,0,3,2] row_mask:0xf bank_mask:0xf\n\t" \
  "v_add_f32_dpp v8, v8, v8 quad_perm:[1,0,3,2] row_mask:0xf bank_mask:0xf\n\ts_nop 0\n\tv_add_f32_dpp v4, v4, v4 quad_perm:[2,3,0,1] row_mask:0xf bank_mask:0xf\n\tv_add_f32_dpp v8, v8, v8 quad_perm:[2,3,0,1] row_mask:0xf bank_mask:0xf\n\t" \
  "s_nop 0\n\tv_add_f32_dpp v4, v4, v4 row_half_mirror row_mask:0xf bank_mask:0xf\n\tv_add_f32_dpp v8, v8, v8 row_half_mirror row_mask:0xf bank_mask:0xf\n\ts_nop 0\n\t" \
  "s_add_u32 s52, s52, 1\n\ts_xor_b32 s53, s52, 0x5bd1e995\n\tv_mul_f32 v6, 0xbacccccd, v4\n\tv_mul_f32 v8, 0x3acccccd, v8\n\t" \
  "v_fma_f32 v8, -v6, v6, v8\n\tv_max_f32 v8, 0, v8\n\tv_add_f32 v8, 0x3727c5ac, v8\n\tv_rsq_f32 v4, v8\n\t" \
  "v_xor_b32 v7, s53, v6\n\tv_xor_b32 v5, s52, v4\n\tbuffer_store_dwordx4 v[4:7], v12, s[88:91], s60 offen sc1\n\ts_cmp_eq_u32 s43, 15\n\t" \
  "s_cbranch_scc1 18f\n\ts_barrier\n\ts_barrier\n\ts_barrier\n\t" \
  "s_barrier\n\ts_barrier\n\tbuffer_load_dwordx4 v[4:7], v13, s[88:91], s60 offen sc1\n\ts_barrier\n\t" \
  "s_branch 12f\n\t11:\n\ts_sub_u32 %[bd], %[bd], 1\n\ts_cbranch_scc1 13f\n\t" \
  "buffer_load_dwordx4 v[4:7], v13, s[88:91], s60 offen sc1\n\t12:\n\ts_waitcnt vmcnt(0)\n\tv_xor_b32 v8, v4, v5\n\t" \
  "v_xor_b32 v9, v6, v7\n\tv_xor_b32 v8, s52, v8\n\tv_xor_b32 v9, s53, v9\n\tv_or_b32 v8, v8, v9\n\t" \
  "v_cmp_ne_u32 vcc, 0, v8\n\ts_cbranch_vccnz 11b\n\t13:\n\tds_write_b32 v14, v4\n\t" \
  "ds_write_b32 v14, v6 offset:512\n\ts_waitcnt lgkmcnt(0)\n\ts_barrier\n\tv_readfirstlane_b32 %[t0], v16\n\t" \
  "v_readfirstlane_b32 %[t1], v17\n\ts_add_u32 %[t0], %[t0], 1\n\ts_barrier\n\ts_branch 9f\n\t" \
  "18:\n\ts_waitcnt vmcnt(0)\n\tv_readfirstlane_b32 %[t0], v16\n\tv_readfirstlane_b32 %[t1], v17\n\t" \
  "s_add_u32 %[t0], %[t0], 1\n\ts_branch 9f\n\t2:\n\ts_cmp_eq_u32 s43, 15\n\t" \
  "s_cbranch_scc1 7f\n\ts_cmp_lt_u32 %[wv], 4\n\ts_cbranch_scc1 3f\n\tv_lshrrev_b32 v2, 3, v1\n\t" \
  "v_and_b32 v3, 7, v1\n\tv_lshrrev_b32 v4, 4, v1\n\tv_xor_b32 v3, v3, v4\n\tv_mul_u32_u24 v2, 1280, v2\n\t" \
  "v_lshl_add_u32 v10, v3, 4, v2\n\tv_xor_b32 v11, 64, v10\n\tv_add_u32 v11, 10240, v11\n\tv_bfe_u32 v2, v1, 2, 3\n\t" \
  "v_mul_u32_u24 v2, 3840, v2\n\tv_lshrrev_b32 v3, 5, v1\n\tv_and_b32 v4, 3, v1\n\tv_lshl_add_u32 v2, v3, 6, v2\n\t" \
  "v_lshl_add_u32 v12, v4, 4, v2\n\tv_add_u32 v13, 0x80, v12\n\ts_sub_u32 s47, %[wv], 4\n\ts_lshl_b32 s54, s47, 13\n\t" \
  "s_lshl_b32 s52, s42, 1\n\ts_add_u32 s52, s52, s47\n\ts_mul_i32 s52, s52, 81920\n\ts_mul_i32 s53, s47, 122880\n\t" \
  "s_lshl_b32 s46, s43, 8\n\ts_add_u32 s53, s53, s46\n\ts_lshl_b32 s46, s41, 1\n\ts_add_u32 s46, s46, s47\n\t" \
  "s_mul_i32 s46, s46, 12800\n\tv_lshlrev_b32 v2, 7, v1\n\tv_add_u32 v14, s46, v2\n\tv_min_u32 v2, 35, v1\n\t" \
  "v_lshlrev_b32 v2, 7, v2\n\ts_add_u32 s46, s46, 8192\n\tv_add_u32 v15, s46, v2\n\ts_mov_b32 s48, 0\n\t" \
  "s_mov_b32 s49, 0\n\ts_waitcnt lgkmcnt(0)\n\tbuffer_load_dwordx2 v[16:17], off, s[88:91], s59 sc1\n\ts_barrier\n\t" \
  "21:\n\ts_add_u32 s51, s54, s49\n\ts_add_u32 m0, s51, 0\n\ts_add_u32 s46, s52, 0\n\t" \
  "buffer_load_dwordx4 v10, s[80:83], s46 offen lds\n\ts_add_u32 m0, s51, 16384\n\ts_add_u32 s47, s53, 0\n\tbuffer_load_dwordx4 v12, s[84:87], s47 offen lds\n\t" \
  "s_add_u32 m0, s51, 1024\n\ts_nop 0\n\tbuffer_load_dwordx4 v11, s[80:83], s46 offen lds\n\ts_add_u32 m0, s51, 17408\n\t" \
  "s_nop 0\n\tbuffer_load_dwordx4 v13, s[84:87], s47 offen lds\n\ts_add_u32 m0, s51, 2048\n\ts_add_u32 s46, s52, 20480\n\t" \
  "buffer_load_dwordx4 v10, s[80:83], s46 offen lds\n\ts_add_u32 m0, s51, 18432\n\ts_add_u32 s47, s53, 30720\n\tbuffer_load_dwordx4 v12, s[84:87], s47 offen lds\n\t" \
  "s_add_u32 m0, s51, 3072\n\ts_nop 0\n\tbuffer_load_dwordx4 v11, s[80:83], s46 offen lds\n\ts_add_u32 m0, s51, 19456\n\t" \
  "s_nop 0\n\tbuffer_load_dwordx4 v13, s[84:87], s47 offen lds\n\ts_add_u32 m0, s51, 4096\n\ts_add_u32 s46, s52, 40960\n\t" \
  "buffer_load_dwordx4 v10, s[80:83], s46 offen lds\n\ts_add_u32 m0, s51, 20480\n\ts_add_u32 s47, s53, 61440\n\tbuffer_load_dwordx4 v12, s[84:87], s47 offen lds\n\t" \
  "s_add_u32 m0, s51, 5120\n\ts_nop 0\n\tbuffer_load_dwordx4 v11, s[80:83], s46 offen lds\n\ts_add_u32 m0, s51, 21504\n\t" \
  "s_nop 0\n\tbuffer_load_dwordx4 v13, s[84:87], s47 offen lds\n\ts_add_u32 m0, s51, 6144\n\ts_add_u32 s46, s52, 61440\n\t" \
  "buffer_load_dwordx4 v10, s[80:83], s46 offen lds\n\ts_add_u32 m0, s51, 22528\n\ts_add_u32 s47, s53, 92160\n\tbuffer_load_dwordx4 v12, s[84:87], s47 offen lds\n\t" \
  "s_add_u32 m0, s51, 7168\n\ts_nop 0\n\tbuffer_load_dwordx4 v11, s[80:83], s46 offen lds\n\ts_add_u32 m0, s51, 23552\n\t" \
  "s_nop 0\n\tbuffer_load_dwordx4 v13, s[84:87], s47 offen lds\n\ts_add_u32 s52, s52, 128\n\ts_add_u32 s53, s53, 245760\n\t" \
  "s_add_u32 s49, s49, 0x8000\n\ts_and_b32 s49, s49, 0x18000\n\ts_add_u32 s48, s48, 1\n\ts_cmp_lt_u32 s48, 3\n\t" \
  "s_cbranch_scc1 21b\n\ts_waitcnt vmcnt(32)\n\ts_barrier\n\ts_cmp_lt_u32 s48, 10\n\t" \
  "s_cbranch_scc1 21b\n\tbuffer_load_dword v18, v14, s[92:95], 0 offen\n\tbuffer_load_dword v19, v15, s[92:95], 0 offen\n\ts_waitcnt vmcnt(18)\n\t" \
  "s_barrier\n\ts_waitcnt vmcnt(2)\n\ts_barrier\n\tv_readfirstlane_b32 %[t0], v16\n\t" \
  "v_readfirstlane_b32 %[t1], v17\n\ts_add_u32 %[t0], %[t0], 1\n\ts_barrier\n\ts_barrier\n\t" \
  "s_branch 9f\n\t7:\n\ts_waitcnt lgkmcnt(0)\n\tbuffer_load_dwordx2 v[16:17], off, s[88:91], s59 sc1\n\t" \
  "s_waitcnt vmcnt(0)\n\tv_readfirstlane_b32 %[t0], v16\n\tv_readfirstlane_b32 %[t1], v17\n\ts_add_u32 %[t0], %[t0], 1\n\t" \
  "s_branch 9f\n\t3:\n\ts_cmp_lg_u32 %[wv], 0\n\ts_cbranch_scc1 31f\n\t" \
  "s_getpc_b64 s[96:97]\n\t.Lpf%=:\n\ts_sub_u32 s96, s96, .Lpf%=-hk_main\n\ts_subb_u32 s97, s97, 0\n\t" \
  "s_mov_b32 m0, 162816\n\tv_lshlrev_b32 v2, 4, v1\n\ts_lshl_b32 s46, s41, 10\n\tv_add_u32 v3, s46, v2\n\t" \
  "v_add_u32 v4, 0x8000, v3\n\tv_min_u32 v4, hk_tail-hk_main-16, v4\n\tglobal_load_lds_dwordx4 v2, s[96:97]\n\tv_add_u32 v2, 0x400, v2\n\t" \
  "global_load_lds_dwordx4 v2, s[96:97]\n\tv_add_u32 v2, 0x400, v2\n\tglobal_load_lds_dwordx4 v2, s[96:97]\n\tv_add_u32 v2, 0x400, v2\n\t" \
  "global_load_lds_dwordx4 v2, s[96:97]\n\tv_add_u32 v2, 0x400, v2\n\tglobal_load_lds_dwordx4 v3, s[96:97]\n\tglobal_load_lds_dwordx4 v4, s[96:97]\n\t" \
  "31:\n\ts_barrier\n\ts_and_b32 s61, %[wv], 1\n\ts_lshr_b32 s62, %[wv], 1\n\t" \
  "v_lshrrev_b32 v2, 5, v1\n\tv_and_b32 v3, 31, v1\n\tv_bfe_u32 v4, v1, 1, 3\n\tv_xor_b32 v4, v4, v2\n\t" \
  "s_lshl_b32 s46, s61, 13\n\tv_lshlrev_b32 v5, 7, v3\n\tv_add_u32 v5, s46, v5\n\tv_lshl_add_u32 v136, v4, 4, v5\n\t" \
  "v_xor_b32 v137, 32, v136\n\tv_xor_b32 v138, 64, v136\n\tv_xor_b32 v139, 0x60, v136\n\tv_bfe_u32 v4, v1, 2, 2\n\t" \
  "v_bfe_u32 v6, v1, 4, 1\n\tv_and_b32 v7, 3, v1\n\tv_lshlrev_b32 v4, 6, v4\n\tv_lshl_add_u32 v4, v6, 5, v4\n\t" \
  "v_lshl_add_u32 v4, v7, 3, v4\n\tv_lshl_add_u32 v4, v2, 11, v4\n\ts_lshl_b32 s46, s62, 10\n\ts_lshl_b32 s47, s61, 9\n\t" \
  "s_add_u32 s46, s46, s47\n\ts_add_u32 s46, s46, 16384\n\tv_add_u32 v140, s46, v4\n\tv_xor_b32 v141, 0x200, v140\n\t" \
  "s_lshl_b32 s51, s62, 8\n\ts_lshl_b32 s47, s61, 7\n\ts_add_u32 s51, s51, s47\n\ts_add_u32 s51, s51, 132096\n\t" \
  "v_and_b32 v7, 15, v1\n\tv_lshl_add_u32 v142, v7, 2, s51\n\tv_cmp_eq_u32 vcc, v7, v6\n\tv_mov_b32 v8, 0x3f803f80\n\t" \
  "v_mul_u32_u24 v9, 7680, v1\n\tv_cndmask_b32 v68, 0, v8, vcc\n\tv_cmp_gt_u32 vcc, 8, v1\n\tv_mov_b32 v69, v68\n\t" \
  "v_bfrev_b32 v10, 1\n\tv_cndmask_b32 v158, v10, v9, vcc\n\tv_mov_b64 v[70:71], v[68:69]\n\ts_lshl_b32 s50, s61, 8\n\t" \
  "s_add_u32 s50, s50, 131072\n\ts_lshl_b32 s47, s42, 1\n\ts_add_u32 s47, s47, s61\n\ts_lshl_b32 s58, s43, 1\n\t" \
  "s_add_u32 s58, s58, s62\n\ts_lshl_b32 s58, s58, 5\n\ts_add_u32 s58, s58, s47\n\ts_lshl_b32 s58, s58, 3\n\t" \
  "s_add_u32 s58, s58, 23134208\n\tv_mov_b64 v[12:13], 0\n\tv_mov_b64 v[14:15], 0\n\tv_mov_b64 v[16:17], 0\n\t" \
  "v_mov_b64 v[18:19], 0\n\tv_mov_b64 v[20:21], 0\n\tv_mov_b64 v[22:23], 0\n\tv_mov_b64 v[24:25], 0\n\t" \
  "v_mov_b64 v[26:27], 0\n\tv_mov_b64 v[28:29], 0\n\tv_mov_b64 v[30:31], 0\n\tv_mov_b64 v[32:33], 0\n\t" \
  "v_mov_b64 v[34:35], 0\n\tv_mov_b64 v[36:37], 0\n\tv_mov_b64 v[38:39], 0\n\tv_mov_b64 v[40:41], 0\n\t" \
  "v_mov_b64 v[42:43], 0\n\tv_mov_b64 v[44:45], 0\n\tv_mov_b64 v[46:47], 0\n\tv_mov_b64 v[48:49], 0\n\t" \
  "v_mov_b64 v[50:51], 0\n\tv_mov_b64 v[52:53], 0\n\tv_mov_b64 v[54:55], 0\n\tv_mov_b64 v[56:57], 0\n\t" \
  "v_mov_b64 v[58:59], 0\n\tv_mov_b64 v[60:61], 0\n\tv_mov_b64 v[62:63], 0\n\tv_mov_b64 v[64:65], 0\n\t" \
  "v_mov_b64 v[66:67], 0\n\ts_waitcnt lgkmcnt(0)\n\tbuffer_load_dwordx2 v[156:157], off, s[88:91], s59 sc1\n\ts_mov_b32 s48, 0\n\t" \
  "s_mov_b32 s49, 0\n\ts_cmp_gt_u32 s43, 9\n\ts_cbranch_scc1 32f\n\tv_lshl_add_u32 v152, v3, 2, s50\n\t" \
  "v_lshl_add_u32 v153, v2, 4, s51\n\tv_xor_b32 v154, 0x80, v153\n\tv_and_b32 v4, 19, v3\n\tv_and_b32 v5, 4, v3\n\t" \
  "v_and_b32 v6, 8, v3\n\tv_lshl_or_b32 v4, v5, 1, v4\n\tv_lshrrev_b32 v6, 1, v6\n\tv_or_b32 v4, v4, v6\n\t" \
  "s_sub_u32 s46, s47, 1\n\ts_and_b32 s46, s46, 31\n\ts_mul_i32 s46, s46, 5632\n\ts_add_u32 s46, s46, 3145728\n\t" \
  "s_mul_i32 s56, s47, 2560\n\ts_sub_u32 s52, s43, 5\n\ts_cmp_lt_u32 s43, 5\n\ts_cselect_b64 vcc, -1, 0\n\t" \
  "s_cselect_b32 s56, s56, s46\n\ts_cselect_b32 s52, s43, s52\n\ts_mov_b32 s46, 180224\n\ts_cselect_b32 s63, 81920, s46\n\t" \
  "s_cselect_b32 s57, 0x3ea52b5b, 1.0\n\tv_cndmask_b32 v4, v4, v3, vcc\n\tv_lshlrev_b32 v4, 4, v4\n\tv_mul_u32_u24 v5, s63, v2\n\t" \
  "v_lshl_add_u32 v6, v2, 10, v4\n\tv_add_u32 v5, 0x800, v5\n\tv_add_u32 v5, v5, v4\n\ts_lshl_b32 s52, s52, 4\n\t" \
  "s_lshl_b32 s46, s62, 3\n\ts_add_u32 s52, s52, s46\n\ts_lshl_b32 s46, s61, 2\n\tv_and_b32 v7, 7, v1\n\t" \
  "v_xor_b32 v7, s46, v7\n\tv_add_u32 v7, s52, v7\n\tv_mul_u32_u24 v8, 205, v7\n\tv_lshrrev_b32 v8, 10, v8\n\t" \
  "v_mul_u32_u24 v9, 5, v8\n\tv_sub_u32 v7, v7, v9\n\tv_cmp_eq_u32 vcc, 2, v7\n\tv_bfe_u32 v9, 24, v7, 1\n\t" \
  "v_bfe_u32 v7, 18, v7, 1\n\tv_lshl_add_u32 v8, v8, 1, v9\n\tv_mul_u32_u24 v8, s63, v8\n\tv_lshl_add_u32 v8, v7, 9, v8\n\t" \
  "s_mov_b32 s55, vcc_lo\n\ts_bitcmp1_b32 s55, 0\n\ts_cselect_b64 vcc, -1, 0\n\tv_readlane_b32 s46, v8, 0\n\t" \
  "v_cndmask_b32 v144, v6, v5, vcc\n\ts_nop 0\n\tv_add_u32 v144, s46, v144\n\ts_bitcmp1_b32 s55, 1\n\t" \
  "s_cselect_b64 vcc, -1, 0\n\tv_readlane_b32 s46, v8, 1\n\tv_cndmask_b32 v145, v6, v5, vcc\n\ts_nop 0\n\t" \
  "v_add_u32 v145, s46, v145\n\ts_bitcmp1_b32 s55, 2\n\ts_cselect_b64 vcc, -1, 0\n\tv_readlane_b32 s46, v8, 2\n\t" \
  "v_cndmask_b32 v146, v6, v5, vcc\n\ts_nop 0\n\tv_add_u32 v146, s46, v146\n\ts_bitcmp1_b32 s55, 3\n\t" \
  "s_cselect_b64 vcc, -1, 0\n\tv_readlane_b32 s46, v8, 3\n\tv_cndmask_b32 v147, v6, v5, vcc\n\ts_nop 0\n\t" \
  "v_add_u32 v147, s46, v147\n\ts_bitcmp1_b32 s55, 4\n\ts_cselect_b64 vcc, -1, 0\n\tv_readlane_b32 s46, v8, 4\n\t" \
  "v_cndmask_b32 v148, v6, v5, vcc\n\ts_nop 0\n\tv_add_u32 v148, s46, v148\n\ts_bitcmp1_b32 s55, 5\n\t" \
  "s_cselect_b64 vcc, -1, 0\n\tv_readlane_b32 s46, v8, 5\n\tv_cndmask_b32 v149, v6, v5, vcc\n\ts_nop 0\n\t" \
  "v_add_u32 v149, s46, v149\n\ts_bitcmp1_b32 s55, 6\n\ts_cselect_b64 vcc, -1, 0\n\tv_readlane_b32 s46, v8, 6\n\t" \
  "v_cndmask_b32 v150, v6, v5, vcc\n\ts_nop 0\n\tv_add_u32 v150, s46, v150\n\ts_bitcmp1_b32 s55, 7\n\t" \
  "s_cselect_b64 vcc, -1, 0\n\tv_readlane_b32 s46, v8, 7\n\tv_cndmask_b32 v151, v6, v5, vcc\n\ts_nop 0\n\t" \
  "v_add_u32 v151, s46, v151\n\tv_mov_b64 v[0:1], 0\n\tv_mov_b64 v[2:3], 0\n\tv_mov_b64 v[4:5], 0\n\t" \
  "v_mov_b64 v[6:7], 0\n\tv_mov_b64 v[8:9], 0\n\tv_mov_b64 v[10:11], 0\n\ts_barrier\n\t" \
  "ds_read_b128 v[72:75], v136 offset:0\n\tds_read_b64_tr_b16 v[80:81], v140 offset:0\n\tds_read_b64_tr_b16 v[82:83], v140 offset:256\n\tds_read_b128 v[76:79], v136 offset:4096\n\t" \
  "ds_read_b64_tr_b16 v[84:85], v141 offset:0\n\tds_read_b64_tr_b16 v[86:87], v141 offset:256\n\tds_read_b128 v[88:91], v137 offset:0\n\tds_read_b64_tr_b16 v[96:97], v140 offset:4096\n\t" \
  "ds_read_b64_tr_b16 v[98:99], v140 offset:4352\n\tds_read_b128 v[92:95], v137 offset:4096\n\tds_read_b64_tr_b16 v[100:101], v141 offset:4096\n\tds_read_b64_tr_b16 v[102:103], v141 offset:4352\n\t" \
  "61:\n\ts_waitcnt lgkmcnt(6)\n\tv_mfma_f32_32x32x16_bf16 v[0:15], v[80:83], v[72:75], v[0:15]\n\tds_read_b128 v[104:107], v138 offset:0\n\t" \
  "ds_read_b64_tr_b16 v[112:113], v140 offset:8192\n\tds_read_b64_tr_b16 v[114:115], v140 offset:8448\n\tv_mfma_f32_32x32x16_bf16 v[16:31], v[80:83], v[76:79], v[16:31]\n\tds_read_b128 v[108:111], v138 offset:4096\n\t" \
  "ds_read_b64_tr_b16 v[116:117], v141 offset:8192\n\tds_read_b64_tr_b16 v[118:119], v141 offset:8448\n\tv_mfma_f32_16x16x32_bf16 v[64:67], v[68:71], v[80:83], v[64:67]\n\tv_mfma_f32_32x32x16_bf16 v[32:47], v[84:87], v[72:75], v[32:47]\n\t" \
  "v_mfma_f32_32x32x16_bf16 v[48:63], v[84:87], v[76:79], v[48:63]\n\ts_waitcnt lgkmcnt(6)\n\tv_mfma_f32_32x32x16_bf16 v[0:15], v[96:99], v[88:91], v[0:15]\n\tds_read_b128 v[120:123], v139 offset:0\n\t" \
  "ds_read_b64_tr_b16 v[128:129], v140 offset:12288\n\tds_read_b64_tr_b16 v[130:131], v140 offset:12544\n\tv_mfma_f32_32x32x16_bf16 v[16:31], v[96:99], v[92:95], v[16:31]\n\tds_read_b128 v[124:127], v139 offset:4096\n\t" \
  "ds_read_b64_tr_b16 v[132:133], v141 offset:12288\n\tds_read_b64_tr_b16 v[134:135], v141 offset:12544\n\tv_mfma_f32_16x16x32_bf16 v[64:67], v[68:71], v[96:99], v[64:67]\n\tv_mfma_f32_32x32x16_bf16 v[32:47], v[100:103], v[88:91], v[32:47]\n\t" \
  "s_add_u32 s50, s49, 0x8000\n\ts_and_b32 s50, s50, 0x18000\n\ts_sub_u32 s49, s50, s49\n\tv_add_u32 v136, s49, v136\n\t" \
  "v_add_u32 v140, s49, v140\n\tv_add_u32 v141, s49, v141\n\tv_add_u32 v137, s49, v137\n\tv_add_u32 v138, s49, v138\n\t" \
  "v_add_u32 v139, s49, v139\n\ts_mov_b32 s49, s50\n\tv_mfma_f32_32x32x16_bf16 v[48:63], v[100:103], v[92:95], v[48:63]\n\ts_cmp_eq_u32 s48, 9\n\t" \
  "s_waitcnt lgkmcnt(0)\n\ts_cbranch_scc1 62f\n\ts_barrier\n\t62:\n\t" \
  "v_mfma_f32_32x32x16_bf16 v[0:15], v[112:115], v[104:107], v[0:15]\n\tds_read_b128 v[72:75], v136 offset:0\n\tds_read_b64_tr_b16 v[80:81], v140 offset:0\n\tds_read_b64_tr_b16 v[82:83], v140 offset:256\n\t" \
  "v_mfma_f32_32x32x16_bf16 v[16:31], v[112:115], v[108:111], v[16:31]\n\tds_read_b128 v[76:79], v136 offset:4096\n\tds_read_b64_tr_b16 v[84:85], v141 offset:0\n\tds_read_b64_tr_b16 v[86:87], v141 offset:256\n\t" \
  "v_mfma_f32_16x16x32_bf16 v[64:67], v[68:71], v[112:115], v[64:67]\n\tv_mfma_f32_32x32x16_bf16 v[32:47], v[116:119], v[104:107], v[32:47]\n\tv_mfma_f32_32x32x16_bf16 v[48:63], v[116:119], v[108:111], v[48:63]\n\tv_mfma_f32_32x32x16_bf16 v[0:15], v[128:131], v[120:123], v[0:15]\n\t" \
  "ds_read_b128 v[88:91], v137 offset:0\n\tds_read_b64_tr_b16 v[96:97], v140 offset:4096\n\tds_read_b64_tr_b16 v[98:99], v140 offset:4352\n\tv_mfma_f32_32x32x16_bf16 v[16:31], v[128:131], v[124:127], v[16:31]\n\t" \
  "ds_read_b128 v[92:95], v137 offset:4096\n\tds_read_b64_tr_b16 v[100:101], v141 offset:4096\n\tds_read_b64_tr_b16 v[102:103], v141 offset:4352\n\tv_mfma_f32_16x16x32_bf16 v[64:67], v[68:71], v[128:131], v[64:67]\n\t" \
  "v_mfma_f32_32x32x16_bf16 v[32:47], v[132:135], v[120:123], v[32:47]\n\ts_add_u32 s48, s48, 1\n\ts_cmp_lt_u32 s48, 10\n\tv_mfma_f32_32x32x16_bf16 v[48:63], v[132:135], v[124:127], v[48:63]\n\t" \
  "s_cbranch_scc1 61b\n\ts_mov_b64 exec, 0xffff\n\ts_nop 1\n\tds_write_b32 v142, v64\n\t" \
  "ds_write_b32 v142, v65 offset:64\n\ts_mov_b64 exec, -1\n\ts_nop 7\n\ts_waitcnt lgkmcnt(0)\n\t" \
  "s_barrier\n\tds_read_b32 v72, v152\n\tds_read_b32 v74, v152 offset:512\n\tds_read_b32 v76, v152 offset:128\n\t" \
  "ds_read_b32 v78, v152 offset:640\n\tds_read_b128 v[80:83], v153 offset:0\n\tds_read_b128 v[84:87], v153 offset:32\n\tds_read_b128 v[88:91], v153 offset:64\n\t" \
  "ds_read_b128 v[92:95], v153 offset:96\n\tds_read_b128 v[96:99], v154 offset:0\n\tds_read_b128 v[100:103], v154 offset:32\n\tds_read_b128 v[104:107], v154 offset:64\n\t" \
  "ds_read_b128 v[108:111], v154 offset:96\n\ts_waitcnt lgkmcnt(8)\n\tv_mul_f32 v72, s57, v72\n\tv_mul_f32 v76, s57, v76\n\t" \
  "s_waitcnt lgkmcnt(7)\n\tv_pk_fma_f32 v[0:1], v[80:81], v[74:75], v[0:1] op_sel_hi:[1,0,1]\n\tv_pk_fma_f32 v[2:3], v[82:83], v[74:75], v[2:3] op_sel_hi:[1,0,1]\n\tv_pk_fma_f32 v[16:17], v[80:81], v[78:79], v[16:17] op_sel_hi:[1,0,1]\n\t" \
  "v_pk_fma_f32 v[18:19], v[82:83], v[78:79], v[18:19] op_sel_hi:[1,0,1]\n\tv_pk_mul_f32 v[0:1], v[0:1], v[72:73] op_sel_hi:[1,0]\n\tv_pk_mul_f32 v[2:3], v[2:3], v[72:73] op_sel_hi:[1,0]\n\tv_pk_mul_f32 v[16:17], v[16:17], v[76:77] op_sel_hi:[1,0]\n\t" \
  "v_pk_mul_f32 v[18:19], v[18:19], v[76:77] op_sel_hi:[1,0]\n\tv_cvt_pk_bf16_f32 v0, v0, v1\n\tv_cvt_pk_bf16_f32 v1, v2, v3\n\tv_cvt_pk_bf16_f32 v2, v16, v17\n\t" \
  "v_cvt_pk_bf16_f32 v3, v18, v19\n\ts_bitcmp1_b32 s55, 0\n\ts_cbranch_scc1 40f\n\tv_permlane32_swap_b32 v0, v2\n\t" \
  "v_permlane32_swap_b32 v1, v3\n\t40:\n\tbuffer_store_dwordx4 v[0:3], v144, s[88:91], s56 offen sc1\n\ts_waitcnt lgkmcnt(6)\n\t" \
  "v_pk_fma_f32 v[4:5], v[84:85], v[74:75], v[4:5] op_sel_hi:[1,0,1]\n\tv_pk_fma_f32 v[6:7], v[86:87], v[74:75], v[6:7] op_sel_hi:[1,0,1]\n\tv_pk_fma_f32 v[20:21], v[84:85], v[78:79], v[20:21] op_sel_hi:[1,0,1]\n\tv_pk_fma_f32 v[22:23], v[86:87], v[78:79], v[22:23] op_sel_hi:[1,0,1]\n\t" \
  "v_pk_mul_f32 v[4:5], v[4:5], v[72:73] op_sel_hi:[1,0]\n\tv_pk_mul_f32 v[6:7], v[6:7], v[72:73] op_sel_hi:[1,0]\n\tv_pk_mul_f32 v[20:21], v[20:21], v[76:77] op_sel_hi:[1,0]\n\tv_pk_mul_f32 v[22:23], v[22:23], v[76:77] op_sel_hi:[1,0]\n\t" \
  "v_cvt_pk_bf16_f32 v4, v4, v5\n\tv_cvt_pk_bf16_f32 v5, v6, v7\n\tv_cvt_pk_bf16_f32 v6, v20, v21\n\tv_cvt_pk_bf16_f32 v7, v22, v23\n\t" \
  "s_bitcmp1_b32 s55, 1\n\ts_cbranch_scc1 41f\n\tv_permlane32_swap_b32 v4, v6\n\tv_permlane32_swap_b32 v5, v7\n\t" \
  "41:\n\tbuffer_store_dwordx4 v[4:7], v145, s[88:91], s56 offen sc1\n\ts_waitcnt lgkmcnt(5)\n\tv_pk_fma_f32 v[8:9], v[88:89], v[74:75], v[8:9] op_sel_hi:[1,0,1]\n\t" \
  "v_pk_fma_f32 v[10:11], v[90:91], v[74:75], v[10:11] op_sel_hi:[1,0,1]\n\tv_pk_fma_f32 v[24:25], v[88:89], v[78:79], v[24:25] op_sel_hi:[1,0,1]\n\tv_pk_fma_f32 v[26:27], v[90:91], v[78:79], v[26:27] op_sel_hi:[1,0,1]\n\tv_pk_mul_f32 v[8:9], v[8:9], v[72:73] op_sel_hi:[1,0]\n\t" \
  "v_pk_mul_f32 v[10:11], v[10:11], v[72:73] op_sel_hi:[1,0]\n\tv_pk_mul_f32 v[24:25], v[24:25], v[76:77] op_sel_hi:[1,0]\n\tv_pk_mul_f32 v[26:27], v[26:27], v[76:77] op_sel_hi:[1,0]\n\tv_cvt_pk_bf16_f32 v8, v8, v9\n\t" \
  "v_cvt_pk_bf16_f32 v9, v10, v11\n\tv_cvt_pk_bf16_f32 v10, v24, v25\n\tv_cvt_pk_bf16_f32 v11, v26, v27\n\ts_bitcmp1_b32 s55, 2\n\t" \
  "s_cbranch_scc1 42f\n\tv_permlane32_swap_b32 v8, v10\n\tv_permlane32_swap_b32 v9, v11\n\t42:\n\t" \
  "buffer_store_dwordx4 v[8:11], v146, s[88:91], s56 offen sc1\n\ts_waitcnt lgkmcnt(4)\n\tv_pk_fma_f32 v[12:13], v[92:93], v[74:75], v[12:13] op_sel_hi:[1,0,1]\n\tv_pk_fma_f32 v[14:15], v[94:95], v[74:75], v[14:15] op_sel_hi:[1,0,1]\n\t" \
  "v_pk_fma_f32 v[28:29], v[92:93], v[78:79], v[28:29] op_sel_hi:[1,0,1]\n\tv_pk_fma_f32 v[30:31], v[94:95], v[78:79], v[30:31] op_sel_hi:[1,0,1]\n\tv_pk_mul_f32 v[12:13], v[12:13], v[72:73] op_sel_hi:[1,0]\n\tv_pk_mul_f32 v[14:15], v[14:15], v[72:73] op_sel_hi:[1,0]\n\t" \
  "v_pk_mul_f32 v[28:29], v[28:29], v[76:77] op_sel_hi:[1,0]\n\tv_pk_mul_f32 v[30:31], v[30:31], v[76:77] op_sel_hi:[1,0]\n\tv_cvt_pk_bf16_f32 v12, v12, v13\n\tv_cvt_pk_bf16_f32 v13, v14, v15\n\t" \
  "v_cvt_pk_bf16_f32 v14, v28, v29\n\tv_cvt_pk_bf16_f32 v15, v30, v31\n\ts_bitcmp1_b32 s55, 3\n\ts_cbranch_scc1 43f\n\t" \
  "v_permlane32_swap_b32 v12, v14\n\tv_permlane32_swap_b32 v13, v15\n\t43:\n\tbuffer_store_dwordx4 v[12:15], v147, s[88:91], s56 offen sc1\n\t" \
  "s_waitcnt lgkmcnt(3)\n\tv_pk_fma_f32 v[32:33], v[96:97], v[74:75], v[32:33] op_sel_hi:[1,0,1]\n\tv_pk_fma_f32 v[34:35], v[98:99], v[74:75], v[34:35] op_sel_hi:[1,0,1]\n\tv_pk_fma_f32 v[48:49], v[96:97], v[78:79], v[48:49] op_sel_hi:[1,0,1]\n\t" \
  "v_pk_fma_f32 v[50:51], v[98:99], v[78:79], v[50:51] op_sel_hi:[1,0,1]\n\tv_pk_mul_f32 v[32:33], v[32:33], v[72:73] op_sel_hi:[1,0]\n\tv_pk_mul_f32 v[34:35], v[34:35], v[72:73] op_sel_hi:[1,0]\n\tv_pk_mul_f32 v[48:49], v[48:49], v[76:77] op_sel_hi:[1,0]\n\t" \
  "v_pk_mul_f32 v[50:51], v[50:51], v[76:77] op_sel_hi:[1,0]\n\tv_cvt_pk_bf16_f32 v32, v32, v33\n\tv_cvt_pk_bf16_f32 v33, v34, v35\n\tv_cvt_pk_bf16_f32 v34, v48, v49\n\t" \
  "v_cvt_pk_bf16_f32 v35, v50, v51\n\ts_bitcmp1_b32 s55, 4\n\ts_cbranch_scc1 44f\n\tv_permlane32_swap_b32 v32, v34\n\t" \
  "v_permlane32_swap_b32 v33, v35\n\t44:\n\tbuffer_store_dwordx4 v[32:35], v148, s[88:91], s56 offen sc1\n\ts_waitcnt lgkmcnt(2)\n\t" \
  "v_pk_fma_f32 v[36:37], v[100:101], v[74:75], v[36:37] op_sel_hi:[1,0,1]\n\tv_pk_fma_f32 v[38:39], v[102:103], v[74:75], v[38:39] op_sel_hi:[1,0,1]\n\tv_pk_fma_f32 v[52:53], v[100:101], v[78:79], v[52:53] op_sel_hi:[1,0,1]\n\tv_pk_fma_f32 v[54:55], v[102:103], v[78:79], v[54:55] op_sel_hi:[1,0,1]\n\t" \
  "v_pk_mul_f32 v[36:37], v[36:37], v[72:73] op_sel_hi:[1,0]\n\tv_pk_mul_f32 v[38:39], v[38:39], v[72:73] op_sel_hi:[1,0]\n\tv_pk_mul_f32 v[52:53], v[52:53], v[76:77] op_sel_hi:[1,0]\n\tv_pk_mul_f32 v[54:55], v[54:55], v[76:77] op_sel_hi:[1,0]\n\t" \
  "v_cvt_pk_bf16_f32 v36, v36, v37\n\tv_cvt_pk_bf16_f32 v37, v38, v39\n\tv_cvt_pk_bf16_f32 v38, v52, v53\n\tv_cvt_pk_bf16_f32 v39, v54, v55\n\t" \
  "s_bitcmp1_b32 s55, 5\n\ts_cbranch_scc1 45f\n\tv_permlane32_swap_b32 v36, v38\n\tv_permlane32_swap_b32 v37, v39\n\t" \
  "45:\n\tbuffer_store_dwordx4 v[36:39], v149, s[88:91], s56 offen sc1\n\ts_waitcnt lgkmcnt(1)\n\tv_pk_fma_f32 v[40:41], v[104:105], v[74:75], v[40:41] op_sel_hi:[1,0,1]\n\t" \
  "v_pk_fma_f32 v[42:43], v[106:107], v[74:75], v[42:43] op_sel_hi:[1,0,1]\n\tv_pk_fma_f32 v[56:57], v[104:105], v[78:79], v[56:57] op_sel_hi:[1,0,1]\n\tv_pk_fma_f32 v[58:59], v[106:107], v[78:79], v[58:59] op_sel_hi:[1,0,1]\n\tv_pk_mul_f32 v[40:41], v[40:41], v[72:73] op_sel_hi:[1,0]\n\t" \
  "v_pk_mul_f32 v[42:43], v[42:43], v[72:73] op_sel_hi:[1,0]\n\tv_pk_mul_f32 v[56:57], v[56:57], v[76:77] op_sel_hi:[1,0]\n\tv_pk_mul_f32 v[58:59], v[58:59], v[76:77] op_sel_hi:[1,0]\n\tv_cvt_pk_bf16_f32 v40, v40, v41\n\t" \
  "v_cvt_pk_bf16_f32 v41, v42, v43\n\tv_cvt_pk_bf16_f32 v42, v56, v57\n\tv_cvt_pk_bf16_f32 v43, v58, v59\n\ts_bitcmp1_b32 s55, 6\n\t" \
  "s_cbranch_scc1 46f\n\tv_permlane32_swap_b32 v40, v42\n\tv_permlane32_swap_b32 v41, v43\n\t46:\n\t" \
  "buffer_store_dwordx4 v[40:43], v150, s[88:91], s56 offen sc1\n\ts_waitcnt lgkmcnt(0)\n\tv_pk_fma_f32 v[44:45], v[108:109], v[74:75], v[44:45] op_sel_hi:[1,0,1]\n\tv_pk_fma_f32 v[46:47], v[110:111], v[74:75], v[46:47] op_sel_hi:[1,0,1]\n\t" \
  "v_pk_fma_f32 v[60:61], v[108:109], v[78:79], v[60:61] op_sel_hi:[1,0,1]\n\tv_pk_fma_f32 v[62:63], v[110:111], v[78:79], v[62:63] op_sel_hi:[1,0,1]\n\tv_pk_mul_f32 v[44:45], v[44:45], v[72:73] op_sel_hi:[1,0]\n\tv_pk_mul_f32 v[46:47], v[46:47], v[72:73] op_sel_hi:[1,0]\n\t" \
  "v_pk_mul_f32 v[60:61], v[60:61], v[76:77] op_sel_hi:[1,0]\n\tv_pk_mul_f32 v[62:63], v[62:63], v[76:77] op_sel_hi:[1,0]\n\tv_cvt_pk_bf16_f32 v44, v44, v45\n\tv_cvt_pk_bf16_f32 v45, v46, v47\n\t" \
  "v_cvt_pk_bf16_f32 v46, v60, v61\n\tv_cvt_pk_bf16_f32 v47, v62, v63\n\ts_bitcmp1_b32 s55, 7\n\ts_cbranch_scc1 47f\n\t" \
  "v_permlane32_swap_b32 v44, v46\n\tv_permlane32_swap_b32 v45, v47\n\t47:\n\tbuffer_store_dwordx4 v[44:47], v151, s[88:91], s56 offen sc1\n\t" \
  "s_branch 33f\n\t32:\n\tv_lshl_add_u32 v152, v2, 4, s50\n\tv_lshl_add_u32 v153, v3, 2, s51\n\t" \
  "v_xor_b32 v154, 0x80, v153\n\ts_mul_i32 s56, s47, 5632\n\ts_add_u32 s56, s56, 3148288\n\ts_mov_b32 s63, 180224\n\t" \
  "s_lshl_b32 s52, s43, 7\n\ts_lshl_b32 s46, s62, 6\n\ts_add_u32 s52, s52, s46\n\ts_sub_u32 s52, s52, 1280\n\t" \
  "s_lshl_b32 s46, s61, 5\n\ts_add_u32 s46, s46, s52\n\tv_add_u32 v4, s46, v3\n\tv_xor_b32 v5, 32, v4\n\t" \
  "v_mul_u32_u24 v6, 384, v2\n\tv_mul_u32_u24 v7, 3277, v4\n\tv_lshrrev_b32 v7, 16, v7\n\tv_mul_u32_u24 v8, 20, v7\n\t" \
  "v_sub_u32 v8, v4, v8\n\tv_mad_u32_u24 v7, v7, s63, v6\n\tv_lshl_add_u32 v144, v8, 4, v7\n\tv_mul_u32_u24 v7, 3277, v5\n\t" \
  "v_lshrrev_b32 v7, 16, v7\n\tv_mul_u32_u24 v8, 20, v7\n\tv_sub_u32 v8, v5, v8\n\tv_mad_u32_u24 v7, v7, s63, v6\n\t" \
  "v_lshl_add_u32 v145, v8, 4, v7\n\ts_add_u32 s46, s52, 19\n\ts_mul_i32 s46, s46, 3277\n\ts_lshr_b32 s46, s46, 16\n\t" \
  "s_add_u32 s52, s52, 64\n\tv_add_u32 v4, s46, v2\n\tv_mul_u32_u24 v5, 20, v4\n\tv_cmp_gt_u32 vcc, s52, v5\n\t" \
  "v_add_u32 v8, 40, v5\n\tv_bfe_u32 v5, v1, 2, 3\n\tv_mul_u32_u24 v5, 384, v5\n\tv_mad_u32_u24 v4, v4, s63, v5\n\t" \
  "v_and_b32 v6, 3, v1\n\tv_lshl_add_u32 v4, v6, 4, v4\n\tv_add_u32 v4, 320, v4\n\tv_cndmask_b32 v146, v10, v4, vcc\n\t" \
  "v_cmp_gt_u32 vcc, s52, v8\n\tv_add_u32 v4, 360448, v4\n\ts_nop 0\n\tv_cndmask_b32 v147, v10, v4, vcc\n\t" \
  "v_cmp_eq_u32 vcc, 0, v6\n\tv_mov_b32 v8, 0x3f803f80\n\ts_nop 0\n\tv_cndmask_b32 v160, 0, v8, vcc\n\t" \
  "v_mov_b32 v161, v160\n\tv_mov_b64 v[162:163], v[160:161]\n\tv_mov_b64 v[0:1], 0\n\tv_mov_b64 v[2:3], 0\n\t" \
  "v_mov_b64 v[4:5], 0\n\tv_mov_b64 v[6:7], 0\n\tv_mov_b64 v[8:9], 0\n\tv_mov_b64 v[10:11], 0\n\t" \
  "s_barrier\n\tds_read_b128 v[72:75], v136 offset:0\n\tds_read_b64_tr_b16 v[80:81], v140 offset:0\n\tds_read_b64_tr_b16 v[82:83], v140 offset:256\n\t" \
  "ds_read_b128 v[76:79], v136 offset:4096\n\tds_read_b64_tr_b16 v[84:85], v141 offset:0\n\tds_read_b64_tr_b16 v[86:87], v141 offset:256\n\tds_read_b128 v[88:91], v137 offset:0\n\t" \
  "ds_read_b64_tr_b16 v[96:97], v140 offset:4096\n\tds_read_b64_tr_b16 v[98:99], v140 offset:4352\n\tds_read_b128 v[92:95], v137 offset:4096\n\tds_read_b64_tr_b16 v[100:101], v141 offset:4096\n\t" \
  "ds_read_b64_tr_b16 v[102:103], v141 offset:4352\n\t51:\n\ts_waitcnt lgkmcnt(6)\n\tv_mfma_f32_32x32x16_bf16 v[0:15], v[72:75], v[80:83], v[0:15]\n\t" \
  "ds_read_b128 v[104:107], v138 offset:0\n\tds_read_b64_tr_b16 v[112:113], v140 offset:8192\n\tds_read_b64_tr_b16 v[114:115], v140 offset:8448\n\tv_mfma_f32_32x32x16_bf16 v[16:31], v[76:79], v[80:83], v[16:31]\n\t" \
  "ds_read_b128 v[108:111], v138 offset:4096\n\tds_read_b64_tr_b16 v[116:117], v141 offset:8192\n\tds_read_b64_tr_b16 v[118:119], v141 offset:8448\n\tv_mfma_f32_16x16x32_bf16 v[64:67], v[68:71], v[80:83], v[64:67]\n\t" \
  "v_mfma_f32_32x32x16_bf16 v[32:47], v[72:75], v[84:87], v[32:47]\n\tv_mfma_f32_32x32x16_bf16 v[48:63], v[76:79], v[84:87], v[48:63]\n\ts_waitcnt lgkmcnt(6)\n\tv_mfma_f32_32x32x16_bf16 v[0:15], v[88:91], v[96:99], v[0:15]\n\t" \
  "ds_read_b128 v[120:123], v139 offset:0\n\tds_read_b64_tr_b16 v[128:129], v140 offset:12288\n\tds_read_b64_tr_b16 v[130:131], v140 offset:12544\n\tv_mfma_f32_32x32x16_bf16 v[16:31], v[92:95], v[96:99], v[16:31]\n\t" \
  "ds_read_b128 v[124:127], v139 offset:4096\n\tds_read_b64_tr_b16 v[132:133], v141 offset:12288\n\tds_read_b64_tr_b16 v[134:135], v141 offset:12544\n\tv_mfma_f32_16x16x32_bf16 v[64:67], v[68:71], v[96:99], v[64:67]\n\t" \
  "v_mfma_f32_32x32x16_bf16 v[32:47], v[88:91], v[100:103], v[32:47]\n\ts_add_u32 s50, s49, 0x8000\n\ts_and_b32 s50, s50, 0x18000\n\ts_sub_u32 s49, s50, s49\n\t" \
  "v_add_u32 v136, s49, v136\n\tv_add_u32 v140, s49, v140\n\tv_add_u32 v141, s49, v141\n\tv_add_u32 v137, s49, v137\n\t" \
  "v_add_u32 v138, s49, v138\n\tv_add_u32 v139, s49, v139\n\ts_mov_b32 s49, s50\n\tv_mfma_f32_32x32x16_bf16 v[48:63], v[92:95], v[100:103], v[48:63]\n\t" \
  "s_cmp_eq_u32 s48, 9\n\ts_waitcnt lgkmcnt(0)\n\ts_cbranch_scc1 52f\n\ts_barrier\n\t" \
  "52:\n\tv_mfma_f32_32x32x16_bf16 v[0:15], v[104:107], v[112:115], v[0:15]\n\tds_read_b128 v[72:75], v136 offset:0\n\tds_read_b64_tr_b16 v[80:81], v140 offset:0\n\t" \
  "ds_read_b64_tr_b16 v[82:83], v140 offset:256\n\tv_mfma_f32_32x32x16_bf16 v[16:31], v[108:111], v[112:115], v[16:31]\n\tds_read_b128 v[76:79], v136 offset:4096\n\tds_read_b64_tr_b16 v[84:85], v141 offset:0\n\t" \
  "ds_read_b64_tr_b16 v[86:87], v141 offset:256\n\tv_mfma_f32_16x16x32_bf16 v[64:67], v[68:71], v[112:115], v[64:67]\n\tv_mfma_f32_32x32x16_bf16 v[32:47], v[104:107], v[116:119], v[32:47]\n\tv_mfma_f32_32x32x16_bf16 v[48:63], v[108:111], v[116:119], v[48:63]\n\t" \
  "v_mfma_f32_32x32x16_bf16 v[0:15], v[120:123], v[128:131], v[0:15]\n\tds_read_b128 v[88:91], v137 offset:0\n\tds_read_b64_tr_b16 v[96:97], v140 offset:4096\n\tds_read_b64_tr_b16 v[98:99], v140 offset:4352\n\t" \
  "v_mfma_f32_32x32x16_bf16 v[16:31], v[124:127], v[128:131], v[16:31]\n\tds_read_b128 v[92:95], v137 offset:4096\n\tds_read_b64_tr_b16 v[100:101], v141 offset:4096\n\tds_read_b64_tr_b16 v[102:103], v141 offset:4352\n\t" \
  "v_mfma_f32_16x16x32_bf16 v[64:67], v[68:71], v[128:131], v[64:67]\n\tv_mfma_f32_32x32x16_bf16 v[32:47], v[120:123], v[132:135], v[32:47]\n\ts_add_u32 s48, s48, 1\n\ts_cmp_lt_u32 s48, 10\n\t" \
  "v_mfma_f32_32x32x16_bf16 v[48:63], v[124:127], v[132:135], v[48:63]\n\ts_cbranch_scc1 51b\n\ts_mov_b64 exec, 0xffff\n\ts_nop 1\n\t" \
  "ds_write_b32 v142, v64\n\tds_write_b32 v142, v65 offset:64\n\ts_mov_b64 exec, -1\n\ts_nop 7\n\t" \
  "s_waitcnt lgkmcnt(0)\n\ts_barrier\n\tds_read_b32 v136, v153\n\tds_read_b32 v138, v154\n\t" \
  "ds_read_b128 v[72:75], v152 offset:0\n\tds_read_b128 v[76:79], v152 offset:512\n\tds_read_b128 v[80:83], v152 offset:32\n\tds_read_b128 v[84:87], v152 offset:544\n\t" \
  "ds_read_b128 v[88:91], v152 offset:64\n\tds_read_b128 v[92:95], v152 offset:576\n\tds_read_b128 v[96:99], v152 offset:96\n\tds_read_b128 v[100:103], v152 offset:608\n\t" \
  "ds_read_b128 v[104:107], v152 offset:128\n\tds_read_b128 v[108:111], v152 offset:640\n\tds_read_b128 v[112:115], v152 offset:160\n\tds_read_b128 v[116:119], v152 offset:672\n\t" \
  "ds_read_b128 v[120:123], v152 offset:192\n\tds_read_b128 v[124:127], v152 offset:704\n\tds_read_b128 v[128:131], v152 offset:224\n\tds_read_b128 v[132:135], v152 offset:736\n\t" \
  "buffer_store_dwordx4 v[160:163], v146, s[88:91], s56 offen sc1\n\tbuffer_store_dwordx4 v[160:163], v147, s[88:91], s56 offen sc1\n\ts_waitcnt lgkmcnt(12)\n\tv_pk_fma_f32 v[0:1], v[76:77], v[136:137], v[0:1] op_sel_hi:[1,0,1]\n\t" \
  "v_pk_fma_f32 v[2:3], v[78:79], v[136:137], v[2:3] op_sel_hi:[1,0,1]\n\tv_pk_fma_f32 v[4:5], v[84:85], v[136:137], v[4:5] op_sel_hi:[1,0,1]\n\tv_pk_fma_f32 v[6:7], v[86:87], v[136:137], v[6:7] op_sel_hi:[1,0,1]\n\tv_pk_mul_f32 v[0:1], v[0:1], v[72:73]\n\t" \
  "v_pk_mul_f32 v[2:3], v[2:3], v[74:75]\n\tv_pk_mul_f32 v[4:5], v[4:5], v[80:81]\n\tv_pk_mul_f32 v[6:7], v[6:7], v[82:83]\n\tv_cvt_pk_bf16_f32 v0, v0, v1\n\t" \
  "v_cvt_pk_bf16_f32 v1, v2, v3\n\tv_cvt_pk_bf16_f32 v2, v4, v5\n\tv_cvt_pk_bf16_f32 v3, v6, v7\n\tbuffer_store_dwordx4 v[0:3], v144, s[88:91], s56 offen offset:0 sc1\n\t" \
  "v_pk_fma_f32 v[32:33], v[76:77], v[138:139], v[32:33] op_sel_hi:[1,0,1]\n\tv_pk_fma_f32 v[34:35], v[78:79], v[138:139], v[34:35] op_sel_hi:[1,0,1]\n\tv_pk_fma_f32 v[36:37], v[84:85], v[138:139], v[36:37] op_sel_hi:[1,0,1]\n\tv_pk_fma_f32 v[38:39], v[86:87], v[138:139], v[38:39] op_sel_hi:[1,0,1]\n\t" \
  "v_pk_mul_f32 v[32:33], v[32:33], v[72:73]\n\tv_pk_mul_f32 v[34:35], v[34:35], v[74:75]\n\tv_pk_mul_f32 v[36:37], v[36:37], v[80:81]\n\tv_pk_mul_f32 v[38:39], v[38:39], v[82:83]\n\t" \
  "v_cvt_pk_bf16_f32 v32, v32, v33\n\tv_cvt_pk_bf16_f32 v33, v34, v35\n\tv_cvt_pk_bf16_f32 v34, v36, v37\n\tv_cvt_pk_bf16_f32 v35, v38, v39\n\t" \
  "buffer_store_dwordx4 v[32:35], v145, s[88:91], s56 offen offset:0 sc1\n\ts_waitcnt lgkmcnt(8)\n\tv_pk_fma_f32 v[8:9], v[92:93], v[136:137], v[8:9] op_sel_hi:[1,0,1]\n\tv_pk_fma_f32 v[10:11], v[94:95], v[136:137], v[10:11] op_sel_hi:[1,0,1]\n\t" \
  "v_pk_fma_f32 v[12:13], v[100:101], v[136:137], v[12:13] op_sel_hi:[1,0,1]\n\tv_pk_fma_f32 v[14:15], v[102:103], v[136:137], v[14:15] op_sel_hi:[1,0,1]\n\tv_pk_mul_f32 v[8:9], v[8:9], v[88:89]\n\tv_pk_mul_f32 v[10:11], v[10:11], v[90:91]\n\t" \
  "v_pk_mul_f32 v[12:13], v[12:13], v[96:97]\n\tv_pk_mul_f32 v[14:15], v[14:15], v[98:99]\n\tv_cvt_pk_bf16_f32 v8, v8, v9\n\tv_cvt_pk_bf16_f32 v9, v10, v11\n\t" \
  "v_cvt_pk_bf16_f32 v10, v12, v13\n\tv_cvt_pk_bf16_f32 v11, v14, v15\n\tbuffer_store_dwordx4 v[8:11], v144, s[88:91], s56 offen offset:768 sc1\n\tv_pk_fma_f32 v[40:41], v[92:93], v[138:139], v[40:41] op_sel_hi:[1,0,1]\n\t" \
  "v_pk_fma_f32 v[42:43], v[94:95], v[138:139], v[42:43] op_sel_hi:[1,0,1]\n\tv_pk_fma_f32 v[44:45], v[100:101], v[138:139], v[44:45] op_sel_hi:[1,0,1]\n\tv_pk_fma_f32 v[46:47], v[102:103], v[138:139], v[46:47] op_sel_hi:[1,0,1]\n\tv_pk_mul_f32 v[40:41], v[40:41], v[88:89]\n\t" \
  "v_pk_mul_f32 v[42:43], v[42:43], v[90:91]\n\tv_pk_mul_f32 v[44:45], v[44:45], v[96:97]\n\tv_pk_mul_f32 v[46:47], v[46:47], v[98:99]\n\tv_cvt_pk_bf16_f32 v40, v40, v41\n\t" \
  "v_cvt_pk_bf16_f32 v41, v42, v43\n\tv_cvt_pk_bf16_f32 v42, v44, v45\n\tv_cvt_pk_bf16_f32 v43, v46, v47\n\tbuffer_store_dwordx4 v[40:43], v145, s[88:91], s56 offen offset:768 sc1\n\t" \
  "s_waitcnt lgkmcnt(4)\n\tv_pk_fma_f32 v[16:17], v[108:109], v[136:137], v[16:17] op_sel_hi:[1,0,1]\n\tv_pk_fma_f32 v[18:19], v[110:111], v[136:137], v[18:19] op_sel_hi:[1,0,1]\n\tv_pk_fma_f32 v[20:21], v[116:117], v[136:137], v[20:21] op_sel_hi:[1,0,1]\n\t" \
  "v_pk_fma_f32 v[22:23], v[118:119], v[136:137], v[22:23] op_sel_hi:[1,0,1]\n\tv_pk_mul_f32 v[16:17], v[16:17], v[104:105]\n\tv_pk_mul_f32 v[18:19], v[18:19], v[106:107]\n\tv_pk_mul_f32 v[20:21], v[20:21], v[112:113]\n\t" \
  "v_pk_mul_f32 v[22:23], v[22:23], v[114:115]\n\tv_cvt_pk_bf16_f32 v16, v16, v17\n\tv_cvt_pk_bf16_f32 v17, v18, v19\n\tv_cvt_pk_bf16_f32 v18, v20, v21\n\t" \
  "v_cvt_pk_bf16_f32 v19, v22, v23\n\tbuffer_store_dwordx4 v[16:19], v144, s[88:91], s56 offen offset:1536 sc1\n\tv_pk_fma_f32 v[48:49], v[108:109], v[138:139], v[48:49] op_sel_hi:[1,0,1]\n\tv_pk_fma_f32 v[50:51], v[110:111], v[138:139], v[50:51] op_sel_hi:[1,0,1]\n\t" \
  "v_pk_fma_f32 v[52:53], v[116:117], v[138:139], v[52:53] op_sel_hi:[1,0,1]\n\tv_pk_fma_f32 v[54:55], v[118:119], v[138:139], v[54:55] op_sel_hi:[1,0,1]\n\tv_pk_mul_f32 v[48:49], v[48:49], v[104:105]\n\tv_pk_mul_f32 v[50:51], v[50:51], v[106:107]\n\t" \
  "v_pk_mul_f32 v[52:53], v[52:53], v[112:113]\n\tv_pk_mul_f32 v[54:55], v[54:55], v[114:115]\n\tv_cvt_pk_bf16_f32 v48, v48, v49\n\tv_cvt_pk_bf16_f32 v49, v50, v51\n\t" \
  "v_cvt_pk_bf16_f32 v50, v52, v53\n\tv_cvt_pk_bf16_f32 v51, v54, v55\n\tbuffer_store_dwordx4 v[48:51], v145, s[88:91], s56 offen offset:1536 sc1\n\ts_waitcnt lgkmcnt(0)\n\t" \
  "v_pk_fma_f32 v[24:25], v[124:125], v[136:137], v[24:25] op_sel_hi:[1,0,1]\n\tv_pk_fma_f32 v[26:27], v[126:127], v[136:137], v[26:27] op_sel_hi:[1,0,1]\n\tv_pk_fma_f32 v[28:29], v[132:133], v[136:137], v[28:29] op_sel_hi:[1,0,1]\n\tv_pk_fma_f32 v[30:31], v[134:135], v[136:137], v[30:31] op_sel_hi:[1,0,1]\n\t" \
  "v_pk_mul_f32 v[24:25], v[24:25], v[120:121]\n\tv_pk_mul_f32 v[26:27], v[26:27], v[122:123]\n\tv_pk_mul_f32 v[28:29], v[28:29], v[128:129]\n\tv_pk_mul_f32 v[30:31], v[30:31], v[130:131]\n\t" \
  "v_cvt_pk_bf16_f32 v24, v24, v25\n\tv_cvt_pk_bf16_f32 v25, v26, v27\n\tv_cvt_pk_bf16_f32 v26, v28, v29\n\tv_cvt_pk_bf16_f32 v27, v30, v31\n\t" \
  "buffer_store_dwordx4 v[24:27], v144, s[88:91], s56 offen offset:2304 sc1\n\tv_pk_fma_f32 v[56:57], v[124:125], v[138:139], v[56:57] op_sel_hi:[1,0,1]\n\tv_pk_fma_f32 v[58:59], v[126:127], v[138:139], v[58:59] op_sel_hi:[1,0,1]\n\tv_pk_fma_f32 v[60:61], v[132:133], v[138:139], v[60:61] op_sel_hi:[1,0,1]\n\t" \
  "v_pk_fma_f32 v[62:63], v[134:135], v[138:139], v[62:63] op_sel_hi:[1,0,1]\n\tv_pk_mul_f32 v[56:57], v[56:57], v[120:121]\n\tv_pk_mul_f32 v[58:59], v[58:59], v[122:123]\n\tv_pk_mul_f32 v[60:61], v[60:61], v[128:129]\n\t" \
  "v_pk_mul_f32 v[62:63], v[62:63], v[130:131]\n\tv_cvt_pk_bf16_f32 v56, v56, v57\n\tv_cvt_pk_bf16_f32 v57, v58, v59\n\tv_cvt_pk_bf16_f32 v58, v60, v61\n\t" \
  "v_cvt_pk_bf16_f32 v59, v62, v63\n\tbuffer_store_dwordx4 v[56:59], v145, s[88:91], s56 offen offset:2304 sc1\n\t33:\n\ts_barrier\n\t" \
  "s_waitcnt vmcnt(0)\n\tv_add_u32 v156, 1, v156\n\tbuffer_store_dwordx2 v[156:157], v158, s[88:91], s58 offen sc1\n\t9:\n\t" \
  "s_nop 4\n\t" \
  ""
#define PA_CLOB "v0", "v1", "v2", "v3", "v4", "v5", "v6", "v7", "v8", "v9", "v10", "v11", "v12", "v13", "v14", "v15", "v16", "v17", "v18", "v19", "v20", "v21", "v22", "v23", "v24", "v25", "v26", "v27", "v28", "v29", "v30", "v31", "v32", "v33", "v34", "v35", "v36", "v37", "v38", "v39", "v40", "v41", "v42", "v43", "v44", "v45", "v46", "v47", "v48", "v49", "v50", "v51", "v52", "v53", "v54", "v55", "v56", "v57", "v58", "v59", "v60", "v61", "v62", "v63", "v64", "v65", "v66", "v67", "v68", "v69", "v70", "v71", "v72", "v73", "v74", "v75", "v76", "v77", "v78", "v79", "v80", "v81", "v82", "v83", "v84", "v85", "v86", "v87", "v88", "v89", "v90", "v91", "v92", "v93", "v94", "v95", "v96", "v97", "v98", "v99", "v100", "v101", "v102", "v103", "v104", "v105", "v106", "v107", "v108", "v109", "v110", "v111", "v112", "v113", "v114", "v115", "v116", "v117", "v118", "v119", "v120", "v121", "v122", "v123", "v124", "v125", "v126", "v127", "v128", "v129", "v130", "v131", "v132", "v133", "v134", "v135", "v136", "v137", "v138", "v139", "v140", "v141", "v142", "v143", "v144", "v145", "v146", "v147", "v148", "v149", "v150", "v151", "v152", "v153", "v154", "v155", "v156", "v157", "v158", "v159", "v160", "v161", "v162", "v163", "s40", "s41", "s42", "s43", "s44", "s45", "s46", "s47", "s48", "s49", "s50", "s51", "s52", "s53", "s54", "s55", "s56", "s57", "s58", "s59", "s60", "s61", "s62", "s63", "s80", "s81", "s82", "s83", "s84", "s85", "s86", "s87", "s88", "s89", "s90", "s91", "s92", "s93", "s94", "s95", "s96", "s97"

#define PK_OUTS , "=&{v[144:147]}"(pkd[0]), "=&{v[148:151]}"(pkd[1]), "=&{v[152:155]}"(pkd[2]), "=&{v[156:159]}"(pkd[3]), "=&{v[160:163]}"(pkd[4]), "=&{v[164:167]}"(pkd[5]), "=&{v[168:171]}"(pkd[6]), "=&{v[172:175]}"(pkd[7]), "=&{v[176:179]}"(pkd[8]), "=&{v[180:183]}"(pkd[9]), "=&{v[184:187]}"(pkd[10]), "=&{v[188:191]}"(pkd[11]), "=&{v[192:195]}"(pkd[12]), "=&{v[196:199]}"(pkd[13]), "=&{v[200:203]}"(pkd[14]), "=&{v[204:207]}"(pkd[15]), "=&{v[208:211]}"(pkd[16]), "=&{v[212:215]}"(pkd[17]), "=&{v[216:219]}"(pkd[18]), "=&{v[220:223]}"(pkd[19]), "=&{v[224:227]}"(pkd[20]), "=&{v[228:229]}"(hh[0]), "=&{v[230:231]}"(hh[1]), "=&{v[232:233]}"(hh[2]), "=&{v[234:235]}"(hh[3]), "=&{v[236:237]}"(hh[4])
#define PK_INS , "{v238}"(um[0]), "{v239}"(um[1]), "{v240}"(um[2]), "{v241}"(ul[0]), "{v242}"(ul[1]), "{v243}"(ul[2]), "{v244}"(gm[0]), "{v245}"(gm[1]), "{v246}"(gm[2]), "{v247}"(hx), [ru] "s"(ru), [rg] "s"(rg), [rx] "s"(rx), [s0] "s"(gu_blk(wv)), [s1] "s"(gu_blk(4 + wv)), [s2] "s"(gu_blk(8 + wv)), [s3] "s"(gu_blk(12 + wv)), [s4] "s"(wv * 160 * 3072 + fs * 96), [s5] "s"(gu_blk(7 + 2 * wv)), [s6] "s"(gu_blk(8 + 2 * wv)), [s7] "s"(jc * 160)
#define PK_Z , "+{v238}"(um[0]), "+{v239}"(um[1]), "+{v240}"(um[2]), "+{v241}"(ul[0]), "+{v242}"(ul[1]), "+{v243}"(ul[2]), "+{v244}"(gm[0]), "+{v245}"(gm[1]), "+{v246}"(gm[2]), "+{v247}"(hx)
template <class T> DI T* kptr(unsigned lo, unsigned hi) { return (T*)(((unsigned long long)hi << 32) | lo); }
extern "C" __global__ __launch_bounds__(512, 2) void hk_main(const u16* x_, const u16* wq_, const u16* wo_, const u16* wg_, const u16* wu_, const u16* wd_,
                                                              char* ws_, u16* out_) {
  TS0();
  u32x16 ka;
  unsigned t0, t1;
  const int wv = __builtin_amdgcn_readfirstlane(threadIdx.x >> 6);
  int budget = EXPV >= 20 ? 64 : 1 << 17;
  {
#define A_FINE
    asm volatile(PA_ASM : "=&{s[64:79]}"(ka), [t0] "=&s"(t0), [t1] "=&s"(t1), [bd] "+s"(budget) A_FINE
                 : [wv] "s"(wv), [kp] "s"(__builtin_amdgcn_kernarg_segment_ptr()), [b] "s"(blockIdx.x) : "memory", "scc", "vcc", PA_CLOB);
    budget = max(budget, 1);
  }
#define PX kptr<const u16>(ka[0], ka[1])
#define PWO kptr<const u16>(ka[4], ka[5])
#define PWS kptr<char>(ka[12], ka[13])
  lds_t sm = (lds_t)smem_g;
  const unsigned smu = (unsigned)(uintptr_t)sm;
  const int b = blockIdx.x, g = b & 7, j = b >> 3;
  const int mt = 4 * (g >> 1) + (j & 3), nt = 8 * (g & 1) + (j >> 2);
  TSM(4);
  const u16 *x = PX, *wo = PWO, *wg = kptr<const u16>(ka[6], ka[7]), *wu = kptr<const u16>(ka[8], ka[9]), *wd = kptr<const u16>(ka[10], ka[11]);
  char* ws = PWS;
  u16* out = kptr<u16>(ka[14], ka[15]);
  const rsrc_t rws = mkr(ws, WS_USED), rx = mkr(x, 2048 * 1280);
  const int rq = j >> 3, jc = j & 7, head = 4 * g + rq, fs = 16 * (g & 1) + (j >> 1);
  const unsigned kv = OFF_KV + head * KVH;
  auto gu_off = [&](int l, int i, bool left) {
    int ci = 64 * i + l, pr = (ci * 10923) >> 16, c = ci - 6 * pr, lr = (pr & 8) ? (pr ^ 4) : pr, e = lr & 7;
    int n = left ? 80 * (lr >> 4) + 64 + 8 * (e >> 2) + 4 * ((lr >> 3) & 1) + (e & 3) : 16 * (e >> 2) + 4 * (lr >> 3) + (e & 3);
    return (unsigned)(n * 3072 + c * 16);
  };
  auto gu_blk = [&](int bl) { return (unsigned)((80 * (bl >> 1) + 32 * (bl & 1)) * 3072 + fs * 96); };
  auto egate = [&](int G, int tk) {
    return (unsigned)((G & 1) * 49152 + ((tk >> 1) & 3) * 3072 + 240 + (G < 2 ? (tk & 1) * 1024 + (tk >> 3) * 512 + 256 : 2048 + (tk & 1) * 512 + (tk >> 3) * 256));
  };
  auto wd_blk = [&](int q, int st) { return (unsigned)((384 * q + 96 * (st / 3) + (st % 3 == 2 ? 32 : 48 * (st % 3))) * 1280 + jc * 160); };
  if (wv >= 4) {
    const int hw = wv - 4, kh = hw >> 1, od = hw & 1, l = lane();
    {
    unsigned wdm[5], wds[5], wom[5], wol[5];
#pragma unroll
    for (int i = 0; i < 5; i++) {
      int ci = 64 * i + l, rr = (ci * 6554) >> 16, c = ci - 10 * rr, lr = (rr & 8) ? (rr ^ 4) : rr, G = lr >> 3, e = lr & 7;
      wdm[i] = (16 * (e >> 2) + 4 * G + (e & 3)) * 1280 + c * 16;
      wds[i] = (48 * (G >> 1) + 4 * (G & 1) + 8 * (e >> 2) + (e & 3)) * 1280 + c * 16;
      wom[i] = (20 * (G >> 1) + (e & 3) + 8 * (e >> 2) + 4 * (G & 1)) * 1280 + c * 16;
      wol[i] = (40 * G + 20 * (e >> 2) + 16 + (e & 3)) * 1280 + c * 16;
    }
    auto woi = [&](int rd) {
      const int pc = 4 * rd + hw, k2 = pc >= 10, i = pc - 10 * k2;
      const char* src = (const char*)wo + jc * 160 + (i < 8 ? (8 * k2 + i) * 40 : (2 * k2 + i - 8) * 160) * 1280;
#pragma unroll
      for (int u = 0; u < 5; u++) { const unsigned o0 = wom[u], o1 = wol[u]; glds(smu + L_WO + pc * 5120 + u * 1024, src, i < 8 ? o0 : o1); }
    };
    auto kvi = [&](int c) {
      unsigned so = smu + (c & 1) * SLOT + hw * 1024;
      const char* src = ws + kv + ((c + jc) & 7) * (4 * PKT) + hw * 1024;
#pragma unroll
      for (int i = 0; i < 5; i++) {
        if (HBD && c == 0 && i == 1 + HBR) __builtin_amdgcn_s_sleep(HBD);
        glds1(so + i * 4096, src + i * 4096, l * 16);
      }
      if (hw < 2) glds1(so + 20480, src + 20480, l * 16);
    };
    const unsigned fa = ((20 * head + 640 + 640 * (l >> 5) + 19 * ((l >> 4) & 1)) >> 6) * 256 + (l & 15) * 16;
    unsigned long long ok = 0;
    auto ensure = [&](int i) {
      const int c2 = 2 * ((i + jc) & 7);
      const unsigned mk = ((7u << c2) | (7u << c2 >> 16)) & 0xffffu, mv = 3u << c2;
      const unsigned long long need = (unsigned long long)(mv * 0x10001u) << 32 | (mk * 0x10001u);
#pragma unroll 1
      while ((ok & need) != need && --budget > 0) {
        fence();
        ok = __builtin_amdgcn_ballot_w64(neq(bl4<DEV>(rws, fa, OFF_F1 + g * 7680), t0, t1) == 0);
      }
    };
    TSM(5);
    if (nt == 15) { __builtin_amdgcn_s_sleep(SPSL); __builtin_amdgcn_s_sleep(SPSL); }
    _Pragma("unroll") for (int i = 0; i < 5; i++) woi(i); SB();
    const int tl = min(l, 59), tn = (4 * j + hw) * 60 + tl, tb = (tn * 6554) >> 20, tw = tn - 160 * tb, th = (tb * 43) >> 9, tr = tb - 12 * th;
    unsigned tgu = (20 * j + 5 * hw + ((tl * 43) >> 9)) * 3072 + (g & 1) * 1536 + (tl - 12 * ((tl * 43) >> 9)) * 128;
    unsigned tda = (24 * th + (tr < 2 ? 2 + 3 * tr : tr < 10 ? tr + 4 : tr + 5)) * 20480 + tw * 128;
    unsigned tdb = (24 * th + (tr < 2 ? 17 - 3 * tr : tr < 8 ? tr + 16 : tr < 10 ? tr - 8 : tr - 7)) * 20480 + tw * 128;
    unsigned gl[3], gm[3], l16 = l * 16;
#pragma unroll
    for (int i = 0; i < 3; i++) { gl[i] = gu_off(l, i, true); gm[i] = gu_off(l, i, false); pin(gl[i]); pin(gm[i]); }
#pragma unroll
    for (int i = 0; i < 5; i++) { pin(wdm[i]); pin(wds[i]); }
    pin(tgu); pin(tda); pin(tdb); pin(l16);
    u32x4 ff = {~0u, ~0u, ~0u, ~0u};
    asm volatile("" : "+v"(ff));
    if (hw == 0) {
      const unsigned fb = ((20 * head + 19 * ((l >> 4) & 1)) >> 6) * 256 + (2 * jc + (l & 1)) * 16;
      u32x4 f0, f1;
#pragma unroll 1
      do {
        fence();
        f0 = bl4<DEV>(rws, fa, OFF_F1 + g * 7680); f1 = bl4<DEV>(rws, fb, OFF_F1 + g * 7680);
      } while (anyl(neq(f1, t0, t1)) && --budget > 0);
      ok = __builtin_amdgcn_ballot_w64(neq(f0, t0, t1) == 0);
      ensure(0);
    }
    bar();
    TSM(6);
    kvi(0);

    const u32x4 fq = bl4<DEV>(rws, fa, OFF_F1 + g * 7680);
    SB();
    if (hw < 2) wait_vm<6 - HBR>(); else wait_vm<5 - HBR>();
    bar();
    SB();
    if (hw == 0) { ok = __builtin_amdgcn_ballot_w64(neq(fq, t0, t1) == 0); ensure(1); }
    wait_vm<0>();
    bar();
    auto fill = [&](int q) {
#pragma unroll
      for (int i = 0; i < 3; i++) __builtin_amdgcn_raw_buffer_store_b128(ff, rws, l16 + i * 1024, OFF_AT + (b * 48 + hw * 12 + 3 * q) * 1024, DEV);
    };
    const rsrc_t rg = mkr(wg, 640 * 3072), rd = mkr(wd, 1536 * 1280);
    const int gb1 = hw == 3 ? 15 : hw + 4;
    u32x4 gp[6], dp[50];
    auto wdl = [&](int i) { const unsigned o0 = wdm[i % 5], o1 = wds[i % 5]; return bl4<0>(rd, (i / 5) % 3 ? o0 : o1, wd_blk(hw, 2 + i / 5)); };
    u32x4 ep[10];
    char* park = ws + (48u << 20) + (4 * b + hw) * 10240;
    auto wel = [&](int i) { return bl4<0>(rd, wdm[i % 5], wd_blk(hw, i / 5)); };
    auto gst = [&]() {
#pragma unroll
      for (int i = 0; i < 6; i++) sts16(sm + ((i < 3 ? hw : gb1) * 3072 + (i % 3) * 1024 + l * 16), gp[i]);
    };
    if (!(HBR && ENS == 1)) kvi(1);
    fill(0); SB();
    touch(smu + L_DUM, wu, tgu, ~0ull); SB();
    if (hw == 0) ensure(2);
    wait_vm<4>(); TSM(7); bar(); TSA(14);
    kvi(2);
    fill(1); SB();
    touch(smu + L_DUM, wg, tgu, ~0ull); SB();
    fill(2); SB();
    if (hw == 0) ensure(3);
    wait_vm<7>(); TSM(8); bar(); TSA(15);
    kvi(3);
    for (int i = 0; i < 3; i++) glds(smu + (16 + hw) * 3072 + i * 1024, (const char*)wg + hw * 160 * 3072 + fs * 96, gl[i]); SB();
    if (hw == 0) ensure(4);
    wait_vm<3>(); TSM(9); bar(); TSA(16);
    kvi(4);
    fill(3); SB();
    if (hw == 0) ensure(5);
    wait_vm<3>(); TSM(10); bar(); TSA(17);
    kvi(5);
    touch(smu + L_DUM, wd, tda, ~0ull); SB();
    for (int i = 0; i < 3; i++) gp[0 + i] = bl4<0>(rg, gm[i], gu_blk(hw)); SB();
    for (int i = 0; i < 3; i++) gp[3 + i] = bl4<0>(rg, gm[i], gu_blk(gb1)); SB();
    if (hw == 0) ensure(6);
    wait_vm<7>(); TSM(11); bar(); TSA(18);
    if (hw == 0 && l < 8) bs2<DEV>(u32x2{t0, t1}, rws, l * 2048, OFF_F2 + b * 8);
    if (hw == 1 && EXPV != 1) code_prefetch(smu + L_DUM, j, l);
    kvi(6);
    touch(smu + L_DUM, wd, tdb, 0x3fffffffull); SB();
    _Pragma("unroll") for (int i = 0; i < 15; i++) dp[i] = wdl(i); SB();
    if (hw == 0) ensure(7);
    wait_vm<16>(); TSM(12); bar(); TSA(19);
    kvi(7);
    u32x4 f0, f1;
    if (hw == 0) { f0 = bl4<DEV>(rws, l * 16, OFF_F2 + g * 2048); f1 = bl4<DEV>(rws, l * 16 + 1024, OFF_F2 + g * 2048); }
    touch(smu + L_DUM, wd, tdb, ~0x3fffffffull); SB();
    _Pragma("unroll") for (int i = 15; i < 30; i++) dp[i] = wdl(i); SB();
    if (hw == 0) {
#pragma unroll 1
      while (anyl(neq(f0, t0, t1) | neq(f1, t0, t1)) && --budget > 0) {
        fence();
        f0 = bl4<DEV>(rws, l * 16, OFF_F2 + g * 2048); f1 = bl4<DEV>(rws, l * 16 + 1024, OFF_F2 + g * 2048);
      }
    }
    wait_vm<16>(); TSM(13); bar(); TSA(20);
    _Pragma("unroll") for (int i = 30; i < 50; i++) dp[i] = wdl(i); SB();
    if (b == 0 && hw == 0 && l == 0) bs2<DEV>(u32x2{t0, t1}, rws, 0, OFF_EP);
    if (EPL != 5) gst();
    __builtin_amdgcn_s_waitcnt(0x0f70);
    TSL(14);
    bar_lgkm();
    if (HX1) bar();
    if (EXPV == 7 && EPL == 5) { TSX(20); __builtin_amdgcn_s_waitcnt(0x0f70); TSX(21); }
    bar();
    if (EPL == 5) {
#pragma unroll
      for (int i = 0; i < 10; i++) sts16(sm + (L_WE + (2 * hw + i / 5) * 5120 + (i % 5) * 1024 + l * 16), ep[i]);
    }
    TSL(15);
    auto edma = [&](int i0 = 0, int i1 = 10, int pp = EPP) {
#pragma unroll
      for (int i = i0; i < i1; i++) {
        if (PRK) glds1(smu + L_WE + hw * 10240 + i * 1024, park + i * 1024, l16);
        else glds(smu + L_WE + (2 * hw + i / 5) * 5120 + (i % 5) * 1024, (const char*)wd + wd_blk(hw, i / 5), wdm[i % 5]);
        if (pp) { SB(); __builtin_amdgcn_s_sleep(EPP); SB(); }
      }
    };
    auto rewdm = [&]() {
      if (PRK) return;
      int l2 = l;
      asm volatile("" : "+v"(l2));
#pragma unroll
      for (int i = 0; i < 5; i++) {
        int ci = 64 * i + l2, rr = (ci * 6554) >> 16, c = ci - 10 * rr, lr = (rr & 8) ? (rr ^ 4) : rr;
        wdm[i] = (16 * ((lr & 7) >> 2) + 4 * (lr >> 3) + (lr & 3)) * 1280 + c * 16;
      }
    };
    if (EPL == 0 && !HG) { edma(); if (EXPV == 7) { TSX(20); wait_vm<0>(); TSX(21); } }
    if (EPL == 4) { __builtin_amdgcn_s_sleep(EPS); edma(); }
    if (EPL >= 6) rewdm();
    if (EPL == 7) { __builtin_amdgcn_s_sleep(EPS); edma(0, EN1, 0); SB(); }
    bar_lgkm();
    if (EPL == 1) edma();
    if (EPL == 6) { __builtin_amdgcn_s_sleep(EPS); edma(0, EN1, 0); SB(); }
    {
      const int G = l >> 4;
      const unsigned wq = (8 * G + ((l & 15) >> 2)) * 96 + (l & 3) * 8 + hw * 15360;
      lds_t p0 = sm + (wq + (G & 1) * 384), p1 = sm + (wq + ((G & 1) ^ 1) * 384);
      const bf16x8 one = B8(u32x4{0x3f803f80u, 0x3f803f80u, 0x3f803f80u, 0x3f803f80u});
      f32x4 c[3];
      float cg[3];
      constexpr int NB = NBS;
      bf16x8 wb[NB];
      auto rdw = [&](int q) { const int o = (q % 15 / 3) * 3072 + (q % 3) * 32 + (q / 15) * L_UP; return trd2(p0 + o, p1 + o); };
#pragma unroll
      for (int q = 0; q < NB; q++) wb[q] = rdw(q);
      SB();
#pragma unroll
      for (int q = 0; q < 30; q++) {
        if (q >= 15 && q < 18) cg[q - 15] = c[q - 15][0];
        c[q % 3] = mm16(one, wb[q % NB], q % 15 < 3 ? f32x4{} : c[q % 3]);
        SB();
        if (q + NB < 30) wb[q % NB] = rdw(q + NB);
        SB();
      }
      const float c0 = G < 2 ? (G ? cg[1] : cg[0]) : (G & 1 ? c[0][0] : cg[2]), c1 = G & 1 ? c[2][0] : c[1][0];
      if (CSL2) {
        *(__attribute__((address_space(3))) float*)(sm + (L_CS + l * 16 + hw * 4)) = c0;
        *(__attribute__((address_space(3))) float*)(sm + (L_CS + 1024 + l * 16 + hw * 4)) = c1;
      } else {
        bs1<0>(f2u(c0), rws, l * 16, OFF_C2 + b * 2048 + hw * 4);
        bs1<0>(f2u(c1), rws, l * 16 + 1024, OFF_C2 + b * 2048 + hw * 4);
      }
    }
    if (CSL2) { TSL(16); bar_lgkm(); bar(); }
    else {
      if (EPL == 2) { SB(); rewdm(); edma(); wait_vm<10>(); } else wait_vm<0>();
      TSL(16); bar();
    }
    if (EPL >= 6) { rewdm(); if (EPS2) __builtin_amdgcn_s_sleep(EPS2); edma(EN1, 10); }
    if (EPL == 3) { rewdm(); if (EPS) __builtin_amdgcn_s_sleep(EPS); if (SU0 == 202) TSX(19); edma(); if (SU0 == 202) { TSX(21); wait_vm<0>(); TSX(3); } }
    if (HG) {
      lds_t hs = sm + (L_WE + hw * 10240 + l * 16);
      u32x4 rw[6];
      bar();
      TSM(1);
      const f32x2 r2 = *(__attribute__((address_space(3))) f32x2*)(hs + (6144 - l * 8));
#pragma unroll
      for (int i = 0; i < 6; i++) rw[i] = lds16(hs + i * 1024);
      asm volatile("s_waitcnt lgkmcnt(0)" : "+v"(rw[0]), "+v"(rw[1]), "+v"(rw[2]), "+v"(rw[3]), "+v"(rw[4]), "+v"(rw[5])::"memory");
      SB();
      rewdm();
      edma();
      TSM(2);
      SB();
      const GelK gk[2] = {gelk(r2[0]), gelk(r2[1])};
      act_out<true>([&](int i) {
        const int tt = i < 4 ? i >> 1 : i & 1;
        return u32x2{gel2(f32x2{blo(rw[i][0]), bhi(rw[i][0])}, f32x2{blo(rw[i][2]), bhi(rw[i][2])}, gk[tt]),
                     gel2(f32x2{blo(rw[i][1]), bhi(rw[i][1])}, f32x2{blo(rw[i][3]), bhi(rw[i][3])}, gk[tt])};
      }, rws, l, OFF_ACT + (8 * (2 * (g >> 1) + (j & 1)) + hw) * 98304 + (fs >> 1) * 3072, fs);
      TSM(3);
    }
    if (!EWT) wait_vm<0>();
    if (EDW) {
      lds_t ps = sm + ((hw >> 1) * L_UP + (hw & 1) * 10240 + l * 16), pe = sm + (L_WE + hw * 10240 + l * 16);
      const unsigned gv = egate(l >> 4, l & 15), gs = OFF_ACT + (8 * g + 4 + rq) * 98304 + hw * 12288;
      unsigned g1 = 0;
      auto wr = [&](int a, int b2) __attribute__((always_inline)) {
#pragma unroll
        for (int i = a; i < b2; i++) sts16(i < 30 ? ps + i / 10 * 20480 + i % 10 * 1024 : i < 40 ? pe + (i - 30) * 1024 : ps + (i - 40) * 1024, dp[i]);
        SB();
      };
      bar();
      wr(0, 10);
      wait_vm<0>();
      bar();
      wr(10, 20);
      bar_lgkm();
      TSL(17);
      wr(20, 40);
      if (PGI == 0) { g1 = bl1<DEV>(rws, gv, gs); SB(); }
      bar_lgkm();
      TSL(18);
      wr(40, 40 + PGW);
      if (PGI == 1) { if (PGS) __builtin_amdgcn_s_sleep(PGS); g1 = bl1<DEV>(rws, gv, gs); SB(); }
      wr(40 + PGW, 50);
      if (PGI == 2) { g1 = bl1<DEV>(rws, gv, gs); SB(); }
      if (PGC == 7 && RARE(anyl(g1 == ~0u))) gate1(rws, gv, gs, budget);
      asm volatile("s_waitcnt lgkmcnt(5)\n\ts_barrier" ::: "memory");
      if (PGC == 8 && RARE(anyl(g1 == ~0u))) gate1(rws, gv, gs, budget);
      bar_lgkm();
      if (PGC > 8) { if (RARE(anyl(g1 == ~0u))) { PCNT(0); gate1(rws, gv, gs, budget); } TSX(20); bar(); }
      TSW(1);
      return;
    }
    bar();
    TSL(17);
    {
      lds_t ps = sm + (6 * hw * 5120 + l * 16), pe = sm + (L_WE + hw * 10240 + l * 16);
      u32x4 eq[10];
      unsigned tch = 0;
      if (ESC == 2) {
        const unsigned gv = egate(l >> 4, l & 15), gs = OFF_ACT + (8 * g + 4 + rq) * 98304 + hw * 12288;
        unsigned g1 = 0;
        if (HPR) __builtin_amdgcn_s_setprio(HPR);
        sfor<0, 9>([&](auto k_) {
          constexpr int k = decltype(k_)::v, c = 10 + 5 * k, e0 = k ? imin(c - 5 + ELA, k <= 4 ? 40 : 50) : 0, e1 = imin(c + ELA, k <= 3 ? 40 : 50);
#pragma unroll
          for (int i = e0; i < e1; i++) sts16(i < 30 ? ps + i * 1024 : i < 40 ? pe + (i - 30) * 1024 : ps + (i - 40) * 1024, dp[i]);
          SB();
          if (k == PGI) { g1 = bl1<DEV>(rws, gv, gs); SB(); }
          if (k == PGC && RARE(anyl(g1 == ~0u))) gate1(rws, gv, gs, budget);
          if (k == HSK) TSX(20);
          if (EBM >> k & 1) asm volatile("s_waitcnt lgkmcnt(%0)\n\ts_barrier" ::"n"(imin((k && k < 8 && !(EBM >> (k + 1) & 1)) ? e1 - c - 5 : e1 - c, 15)) : "memory");
        });
      } else if (ESC) {
        auto wr = [&](int a, int b2) __attribute__((always_inline)) {
#pragma unroll
          for (int i = a; i < b2; i++) sts16(i < 30 ? ps + i * 1024 : i < 40 ? pe + (i - 30) * 1024 : ps + (i - 40) * 1024, dp[i]);
          SB();
        };
        const unsigned gv = egate(l >> 4, l & 15), gs = OFF_ACT + (8 * g + 4 + rq) * 98304 + hw * 12288;
        unsigned g1 = 0;
        wr(0, EA);
        asm volatile("s_waitcnt lgkmcnt(%0)" ::"n"(EA - 20) : "memory");
        TSL(18);
        bar();
        wr(EA, 30);
#pragma unroll
        for (int i = 0; i < 10; i++) eq[i] = lds16(pe + i * 1024);
        SB();
        wr(30, 30 + EB);
        asm volatile("s_waitcnt lgkmcnt(%0)" ::"n"(10 + EB) : "memory");
        bar();
        TSL(19);
        if (PGI == 0) { g1 = bl1<DEV>(rws, gv, gs); SB(); }
        wr(30 + EB, 40 + EC);
        asm volatile("s_waitcnt lgkmcnt(%0)" ::"n"(EC) : "memory");
        bar();
        if (PGI == 1) { g1 = bl1<DEV>(rws, gv, gs); SB(); }
        wr(40 + EC, 50);
        if (ZD2) { asm volatile("s_waitcnt lgkmcnt(5)" ::: "memory"); bar(); }
        if (PGI == 2) { g1 = bl1<DEV>(rws, gv, gs); SB(); }
        bar_lgkm();
        if (PGI == 3) { g1 = bl1<DEV>(rws, gv, gs); SB(); }
        if (RARE(anyl(g1 == ~0u))) gate1(rws, gv, gs, budget);
        TSX(20);
        bar();
#pragma unroll
        for (int i = 0; i < 10; i++) sts16(pe + i * 1024, eq[i]);
        SB();
        bar();
#pragma unroll
        for (int i = 0; i < 10; i++) sts16(ps + i * 1024, dp[i]);
        SB();
        if (ZF2) { asm volatile("s_waitcnt lgkmcnt(10)" ::: "memory"); bar(); }
        bar_lgkm();
      } else {
#pragma unroll
      for (int i = 0; i < 10; i++) eq[i] = lds16(pe + i * 1024);
      SB();
#pragma unroll
      for (int i = 0; i < 30; i++) sts16(ps + i * 1024, dp[i]);
      SB();
      asm volatile("s_waitcnt lgkmcnt(0)" ::: "memory");
      TSL(18);
      bar();
#pragma unroll
      for (int i = 0; i < 10; i++) sts16(pe + i * 1024, dp[30 + i]);
      SB();
      bar();
      TSL(19);
#pragma unroll
      for (int i = 0; i < 10; i++) sts16(ps + i * 1024, dp[40 + i]);
      SB();
      asm volatile("s_waitcnt lgkmcnt(10)" ::: "memory");
      bar();
      bar_lgkm();
      if (HGATE) gate1(rws, egate(l >> 4, l & 15), OFF_ACT + (8 * g + 4 + rq) * 98304 + hw * 12288, budget);
      TSX(20);
      if (ETCH) {
        const int lt = min(l, 23), t2 = lt >= 12;
        unsigned d = bl1<DEV>(rws, t2 * 49152 + (jc * 12 + lt - 12 * t2) * 128, OFF_ACT + (8 * g + 4 + rq) * 98304 + hw * 12288);
        if (ETCH > 1) asm volatile("" ::"v"(d) : "memory");
        SB();
        tch = d;
      }
      bar();
#pragma unroll
      for (int i = 0; i < 20; i++) sts16((i < 10 ? pe : ps) + (i % 10) * 1024, i < 10 ? eq[i] : dp[i - 10]);
      bar_lgkm();
      }
      if (ETCH) asm volatile("" ::"v"(tch) : "memory");
    }
    TSW(1);
    return;
    }
  }
  const int wm = wv & 1, kh = wv >> 1;
  const int T32 = 8 * g + 4 * wm + rq;
#define WFR() const int tok = 32 * T32 + 16 * kh + tk; \
  const unsigned wr = (8 * G + (tk >> 2)) * 160 + (l & 3) * 8, w0 = wr + (G & 1) * 640, w1 = wr + ((G & 1) ^ 1) * 640;
  u32x4 pkd[21];
  u32x2 hh[5];
  {
#define KRV(h, r) const unsigned ka = l * 16, ra = h ? 2560 + 320 : 2048 + r * 16, va = 2560 + h * VH + (r <= 21 ? r : 21) * 16;
    u32x4 qA, qB;
    u32x2 qrA, qrB;
    f32x16 AO, BO;
    {
    LANES()
    KRV(h, r)
    unsigned um[3], ul[3], gm[3], hx = (32 * T32 + 16 * kh + tk) * 1280 + G * 8;
#pragma unroll
    for (int i = 0; i < 3; i++) { um[i] = gu_off(l, i, false); ul[i] = gu_off(l, i, true); gm[i] = um[i]; }
    f32x16 Bhi;
    u32x16 V;
    unsigned kp = smu + ka, rp = smu + ra, vp = smu + va, sc, sd, se, ix = 0xDDDDu;
    asm volatile(ZERO_ASM : "={v[0:15]}"(AO), "={v[16:31]}"(BO), "={v[80:95]}"(Bhi), "={v[108:123]}"(V), "+{v140}"(ix), "+{v141}"(kp), "+{v142}"(rp), "+{v143}"(vp) PK_Z);
    TSM(5);
    bar();
    const unsigned qp = OFF_Q + head * QH + (4 * jc + wv) * 2560, kq = kv + ((4 * jc - 1) & 31) * PKT;
    qA = bl4<DEV>(rws, ka, qp);
    u32x4 k0 = bl4<DEV>(rws, ka, kq), kr = bl4<DEV>(rws, ra, kq), qr = bl4<DEV>(rws, r * 16 + 2048, qp);
    qB = bl4<DEV>(rws, ka + 1024, qp);
    u32x4 k1 = bl4<DEV>(rws, ka + 1024, kq), kr2;
    SB();
    qrA = lo2(qr); qrB = hi2(qr);
    if (h) { qrA = u32x2{0, 0}; qrB = u32x2{0, 0}; if (!NOFIX) kr = u32x4{0x3f803f80u, 0x3f803f80u, 0x3f803f80u, 0x3f803f80u}; }
    const f32x16 z = {};
    f32x16 Alo = mm8(lo2(kr), qrA, mm32(B8(k0), B8(qA), z));
    f32x16 Blo = mm8(lo2(kr), qrB, mm32(B8(k0), B8(qB), z));
    f32x16 Ahi = mm32(B8(k1), B8(qA), z);
    TSM(6);
    unsigned ma = pk(-max32(max16(Alo, -INFINITY)), 0.f), mb = pk(-max32(max16(Blo, -INFINITY)), 0.f);
    if (h) { qrA[0] = ma; qrB[0] = mb; }
    const float fa = blo(ma);
#pragma unroll
    for (int i = 0; i < 16; i++) Alo[i] += fa;
    SB();
    bar();
    SB();
    TSM(7);
    const rsrc_t rg = mkr(wg, 640 * 3072), ru = mkr(wu, 640 * 3072);
    u32x8 K = __builtin_shufflevector(k0, k1, 0, 1, 2, 3, 4, 5, 6, 7);
    asm volatile(ATT_ASM
                 : "+{v[0:15]}"(AO), "+{v[16:31]}"(BO), "+{v[32:47]}"(Alo), "+{v[48:63]}"(Ahi), "+{v[64:79]}"(Blo), "+{v[80:95]}"(Bhi), "+{v[108:123]}"(V),
                   "+{v[124:131]}"(K), "+{v[132:135]}"(kr), "=&{v[136:139]}"(kr2), "+{v141}"(kp), "+{v142}"(rp), "+{v143}"(vp), [c] "=&s"(sc), [d] "=&s"(sd), [e] "=&s"(se) PK_OUTS
                 : "{v[96:99]}"(qA), "{v[100:103]}"(qB), "{v[104:105]}"(qrA), "{v[106:107]}"(qrB), "{v140}"(ix) PK_INS
                 : "memory", "scc");
    }
    TSM(8);
    LANES()
    if (RARE(anyl(!(mag12(BO, mag12(AO, 0.f)) < 1e30f)))) {
      KRV(h, r)
      AO = attn_exact(qA, qrA, rws, kv, ka, ra, va, h);
      BO = attn_exact(qB, qrB, rws, kv, ka, ra, va, h);
    }
    asm volatile("" : "+{v[0:15]}"(AO), "+{v[16:31]}"(BO));
    const unsigned om = ((r >> 4) * 20 + 10 * (head >> 4) + ((head >> 1) & 7)) * 1024 + (2 * (head & 1) + h) * 256 + tk * 16;
    const unsigned ol = ((r >> 4) * 20 + 10 * (head >> 4) + 8 + ((head >> 3) & 1)) * 1024 + (head & 1) * 512 + ((head >> 1) & 3) * 128 + tk * 8;
    auto put = [&](const f32x16& O, int T) {
      const float inv = __builtin_amdgcn_rcpf(swap32(O[8])[1]);
      u32x4 o = {pkc(O[0] * inv, O[1] * inv), pkc(O[2] * inv, O[3] * inv), pkc(O[4] * inv, O[5] * inv), pkc(O[6] * inv, O[7] * inv)};
      u32x2 o2 = {pkc(O[8] * inv, O[9] * inv), pkc(O[10] * inv, O[11] * inv)};
      bs4<DEV>(o, rws, om, OFF_AT + T * 40960);
      if (!h) bs2<DEV>(o2, rws, ol, OFF_AT + T * 40960);
    };
    put(AO, 8 * jc + 2 * wv);
    put(BO, 8 * jc + 2 * wv + 1);
    if (PKL) {
      SB();
      const rsrc_t rg = mkr(wg, 640 * 3072), ru = mkr(wu, 640 * 3072);
#pragma unroll
      for (int t = 0; t < 5; t++) hh[t] = bl2<0>(rx, (32 * T32 + 16 * kh + tk) * 1280 + G * 8 + t * 32, jc * 160);
#pragma unroll
      for (int i = 0; i < PKN; i++)
        pkd[i] = i < 12 ? bl4<0>(ru, gu_off(l, i % 3, false), gu_blk(4 * (i / 3) + wv)) : i < 15 ? bl4<0>(ru, gu_off(l, i % 3, true), wv * 160 * 3072 + fs * 96)
                                                                                                    : bl4<0>(rg, gu_off(l, i % 3, false), gu_blk(7 + 2 * wv + (i - 15) / 3));
      SB();
    }
  }
  const int TA = 8 * (2 * (g >> 1) + (j & 1)) + wv;
  u32x2 st[4][2];
  auto stld = [&](int l) {
#pragma unroll
    for (int i = 0; i < 8; i++) st[i >> 1][i & 1] = bl2<DEV>(rws, (l >> 4) * 256 + (l & 15) * 8 + (i & 1) * 128 + (i & 2) * 512, OFF_ST + (TA + (i & 4)) * 2048);
  };
  {
    LANES() WFR()
    (void)tok;
    bar();
    TSM(9);
    PCNT(1);
    const unsigned ab = OFF_AT + T32 * 40960 + kh * 10240;
    const unsigned go = (G & 1) * 20480 + (G < 2 ? (tk >> 1) * 1024 + (2 * (tk & 1) + 1) * 256 + 240 : (8 + (tk >> 3)) * 1024 + (tk & 1) * 512 + ((tk >> 1) & 3) * 128 + 120);
    lds_t p0 = sm + (L_WO + kh * 51200 + w0), p1 = sm + (L_WO + kh * 51200 + w1);
    f32x4 acc[2][5];
    constexpr int NB = NBC;
    Fr wb[NB];
    auto wpre = [&]() {
#pragma unroll
      for (int q = 0; q < NB - 1; q++) { wb[q].a = trd(p0 + (q / 5) * 5120 + (q % 5) * 32); wb[q].b = trd(p1 + (q / 5) * 5120 + (q % 5) * 32); }
      SB();
    };
    u32x4 a[2][10];
    const unsigned ao[2] = {ab + kh * 20480, ab + (kh ^ 1) * 20480};
    auto hin = [&]() {
#pragma unroll
      for (int t = 0; t < 5; t++) { acc[0][t] = f32x4{blo(hh[t][0]), bhi(hh[t][0]), blo(hh[t][1]), bhi(hh[t][1])}; asm volatile("" : "+v"(acc[0][t])); }
    };
    hin();
    auto lds_ = [&](int i) {
#pragma unroll
      for (int tt = 0; tt < 2; tt++) {
        if (i < 8) a[tt][i] = bl4<DEV>(rws, l * 16 + (i & 3) * 1024, ao[tt] + (i >> 2) * 4096);
        else a[tt][i] = cat2(bl2<DEV>(rws, l * 8 + (i & 1) * 1024, ao[tt] + 8192), bl2<DEV>(rws, l * 8 + (i & 1) * 1024 + 512, ao[tt] + 8192));
      }
    };
    {
      if (CSL) __builtin_amdgcn_s_sleep(CSL);
      gate1(rws, go, ab, budget);
      TSM(10);
      PCNT(2);
#pragma unroll
      for (int i = 0; i < 10; i++) lds_(i);
    }
    if (PKL && PKN < 21) {
      SB();
      const rsrc_t rg = mkr(wg, 640 * 3072), ru = mkr(wu, 640 * 3072);
#pragma unroll
      for (int i = PKN; i < 21; i++)
        pkd[i] = i < 12 ? bl4<0>(ru, gu_off(l, i % 3, false), gu_blk(4 * (i / 3) + wv)) : i < 15 ? bl4<0>(ru, gu_off(l, i % 3, true), wv * 160 * 3072 + fs * 96)
                                                                                                    : bl4<0>(rg, gu_off(l, i % 3, false), gu_blk(7 + 2 * wv + (i - 15) / 3));
      SB();
    }
    if (HX1) bar();
    wpre();
    lds_t rs_ = sm + (21504 + 6144 * (wv ^ 2) + l * 16), rr_ = sm + (21504 + 6144 * wv + l * 16);
#pragma unroll
    for (int q = 0; q < 45; q++) {
      const int i = q / 5, t = q % 5, n = q + NB - 1, o = (n / 5) * 5120 + (n % 5) * 32;
      const f32x4 z = {};
      acc[1][t] = mm16(F8(wb[q % NB]), B8(a[1][i]), i ? acc[1][t] : z);
      SB();
      if (n < 50) wb[n % NB].a = trd(p0 + o);
      SB();
      acc[0][t] = mm16(F8(wb[q % NB]), B8(a[0][i]), acc[0][t]);
      SB();
      if (n < 50) wb[n % NB].b = trd(p1 + o);
      SB();
    }
#pragma unroll
    for (int t = 0; t < 5; t++) { acc[1][t] = mm16(F8(wb[(45 + t) % NB]), B8(a[1][9]), acc[1][t]); SB(); }
#pragma unroll
    for (int t = 0; t < 5; t++) {
      acc[0][t] = mm16(F8(wb[(45 + t) % NB]), B8(a[0][9]), acc[0][t]);
      SB();
      stsf4(rs_ + t * 1024, acc[1][t]);
      SB();
    }
    if (RARE(anyl(isn(acc[0][0][0] + acc[1][0][0])) || (FRD == 4 && (b & 1)))) {
      RCNT(19);
#pragma unroll
      for (int t = 0; t < 5; t++) hh[t] = bl2<0>(mkr(x, 2048 * 1280), tok * 1280 + G * 8 + t * 32, jc * 160);
      hin();
#pragma unroll
      for (int t = 0; t < 5; t++) acc[1][t] = f32x4{};
#pragma unroll 1
      for (int i = 0; i < 10; i++) {
        u32x4 c[2];
#pragma unroll
        for (int tt = 0; tt < 2; tt++) {
          if (i < 8) c[tt] = fetch16(rws, l * 16, ao[tt] + i * 1024, budget);
          else c[tt] = cat2(fetch8(rws, l * 8, ao[tt] + i * 1024, budget), fetch8(rws, l * 8 + 512, ao[tt] + i * 1024, budget));
        }
        W5 f = rd5(p0 + i * 5120, p1 + i * 5120);
        step5(acc, f, c[0], c[1], p0, p1, false, false);
      }
#pragma unroll
      for (int t = 0; t < 5; t++) stsf4(rs_ + t * 1024, acc[1][t]);
    }
    TSM(11);
    bar_lgkm();
    f32x4 pt[5];
#pragma unroll
    for (int t = 0; t < 5; t++) { pt[t] = ldsf4(rr_ + t * 1024); SB(); }
    auto pkw = [&](int i) { sts16(sm + ((i < 15 ? L_UP + (4 * (i / 3) + wv) * 3072 + (i % 3) * 1024 : (7 + 2 * wv) * 3072 + (i - 15) * 1024) + l * 16), pkd[i]); };
#pragma unroll
    for (int i = 0; i < PKE; i++) pkw(i);
    SB();
#pragma unroll
    for (int t = 0; t < 5; t++) {
      const f32x4 o = acc[0][t] + pt[t];
      hh[t] = u32x2{pkc(o[0], o[1]), pkc(o[2], o[3])};
    }
    const unsigned hb = OFF_H + (2 * T32 + kh) * 20480;
    bs4<DEV>(cat2(hh[0], hh[1]), rws, l * 16, hb + jc * 2048);
    bs4<DEV>(cat2(hh[2], hh[3]), rws, l * 16 + 1024, hb + jc * 2048);
    {
      unsigned a0 = hh[4][0], c0 = a0, a1 = hh[4][1], c1 = a1;
      swp32(a0, c0); swp32(a1, c1);
      if (G < 2) bs4<DEV>(u32x4{a0, a1, c0, c1}, rws, l * 16, hb + 16384 + jc * 512);
    }
    {
      const bf16x8 one = B8(u32x4{0x3f803f80u, 0x3f803f80u, 0x3f803f80u, 0x3f803f80u});
      const bf16x8 f0 = B8(cat2(hh[0], hh[1])), f1 = B8(cat2(hh[2], hh[3])), f2 = B8(cat2(hh[4], u32x2{0u, 0u}));
      const f32x4 sq = mm16(f2, f2, mm16(f1, f1, mm16(f0, f0, f32x4{}))), sum = mm16(one, f2, mm16(one, f1, mm16(one, f0, f32x4{})));
      const float q0 = (l & 1) ? sq[1] : sq[0], q1 = (l & 1) ? sq[3] : sq[2];
      if (G == (tk >> 2)) bs2<DEV>(u32x2{min(f2u(sum[0]), 0xFFFFFFFEu), min(f2u((l & 2) ? q1 : q0), 0xFFFFFFFEu)}, rws, tk * 8, OFF_ST + ((2 * T32 + kh) * 8 + jc) * 128);
    }
    TSM(12);
#pragma unroll
    for (int i = PKE; i < 21; i++) {
      if (DEP == i + 1) { SB(); stld(l); SB(); }
      pkw(i);
    }
  }
  u32x4 ea[2][12];
  const unsigned eb = OFF_ACT + (8 * g + rq) * 98304 + wv * 12288;
  Fr wb0[10];
  auto est = [](int hf, int p) { return ESC == 2 ? p : hf ? (p < 4 ? p + 8 : p < 8 ? p : p - 8) : (p < 4 ? p ^ 2 : p); };
  auto elda = [&](int l, int hf, int tt, int p) { const int i = est(hf, p); return bl4<DEV>(rws, l * 16 + (i & 3) * 1024, eb + hf * (4 * 98304) + tt * 49152 + (i >> 2) * 4096); };
  {
    LANES()
    u32x4 cs[6];
#pragma unroll
    for (int t = 0; t < 5; t++) asm volatile("" : "+v"(hh[t]));
    if (SU0 == 201) TSX(20);
    bar_lgkm();
    if (SU0 == 201) TSX(21);
    constexpr int WIN = 20;
    u32x4 a[WIN];
    const unsigned hb = OFF_H + TA * 40960;
    auto lda = [&](int q) { if (LDX == 1) q %= 20; if (LDX == 2 && q >= 20) return a[q % WIN]; const int s2 = q % 40 >> 1; return bl4<DEV>(rws, l * 16 + (s2 & 3) * 1024, hb + (q >= 40 ? 163840 : 0) + (q & 1) * 20480 + (s2 >> 2) * 4096); };
    auto stbad = [&]() {
      unsigned m = 0;
#pragma unroll
      for (int i = 0; i < 8; i++) m = max(m, max(st[i >> 1][i & 1][0], st[i >> 1][i & 1][1]));
      return anyl(m == ~0u);
    };
    if (DSL) __builtin_amdgcn_s_sleep(DSL);
    if (!DEP || stbad()) {
      PCNT(2);
#pragma unroll 1
      do {
        fence();
        stld(l);
        if (DSP) {
          SB();
#pragma unroll
          for (int q = 0; q < DP0; q++) a[q] = lda(q);
          SB();
        }
        if (!stbad()) break;
      } while (--budget > 0);
      if (DEP) --budget;
    }
    TSM(13);
    PCNT(3);
    if (DPR) __builtin_amdgcn_s_setprio(DPR);
    unsigned tch = 0, tca = 0;
    if (DTC) {
      constexpr int NL = (80 - DT0) / 2;
      const int n = min(l, NL - 1) + NL * (j >> 1), q2 = (n >> 3) + DT0;
      tca = (q2 >= 40 ? 163840 - 20480 : 0) + (q2 & 1) * 20480 + (q2 >> 1) * 1024 + (n & 7) * 128;
    }
    if (!DSP) {
#pragma unroll
      for (int q = 0; q < DP0; q++) {
        a[q] = lda(q);
      }
    }
    SB();
    const unsigned wq = (8 * G + (tk >> 2)) * 96 + (l & 3) * 8;
    lds_t p0 = sm + (wq + (G & 1) * 384), p1 = sm + (wq + ((G & 1) ^ 1) * 384), u0 = p0 + L_UP, u1 = p1 + L_UP;
    asm volatile("" : "+v"(u0), "+v"(u1));
    constexpr int NB = NBD;
    Fr wb[NB];
    auto rdh = [&](int f, int s) {
      const int o = (f / 6 % 20) * 3072 + (f % 6 >> 1) * 32;
      if (s) wb[f % NB].b = trd((f & 1 ? u1 : p1) + o); else wb[f % NB].a = trd((f & 1 ? u0 : p0) + o);
    };
#pragma unroll
    for (int f = 0; f < NB - 1; f++) { rdh(f, 0); rdh(f, 1); }
    float rs[4];
    u32x2 mf[4];
#pragma unroll
    for (int i = 0; i < 4; i++) {
      const float sum = sum16(sum32(u2f(st[i][0][0]) + u2f(st[i][1][0]))), sq = sum16(sum32(u2f(st[i][0][1]) + u2f(st[i][1][1])));
      const float mean = sum * (1.f / 640.f);
      rs[i] = __builtin_amdgcn_rsqf(fmaxf(sq * (1.f / 640.f) - mean * mean, 0.f) + 1e-5f);
      const unsigned mh = pk(-mean, -mean);
      const float ml = -mean - blo(mh);
      mf[i] = u32x2{mh, pk(ml, ml)};
      asm volatile("" : "+v"(rs[i]), "+v"(mf[i]));
    }
    SB();
#pragma unroll
    for (int q = DP0; q < WIN; q++) a[q] = lda(q);
    unsigned cv[6];
    auto cfr = [&](int u) { return u32x2{cv[u], cv[u]}; };
    auto fold = [&](int tl, int tt, int u, f32x4 c) {
      return __builtin_amdgcn_mfma_f32_4x4x4bf16_1k(__builtin_bit_cast(s16x4, cfr(u)), __builtin_bit_cast(s16x4, mf[2 * tl + tt]), c, 0, 0, 0);
    };
    auto cvt = [&](int u) {
      const float c = (u2f(cs[u][0]) + u2f(cs[u][1])) + (u2f(cs[u][2]) + u2f(cs[u][3]));
      const unsigned hi = pk(c, 0.f);
      cv[u] = hi | (pk(c - blo(hi), 0.f) << 16);
      pin(cv[u]);
    };
    if (CS0) {
      SB();
      bar();
#pragma unroll
      for (int i = 0; i < 6; i++) cs[i] = lds16(sm + (L_CS + (l >> 4) * 64 + (l & 3) * 16) + ((i >> 1) + 3 * (i & 1)) * 256);
#pragma unroll
      for (int i = 0; i < 6; i++) cvt(i);
      SB();
      bar();
    }
    auto tile = [&](auto tl_) __attribute__((always_inline)) {
      constexpr int tl = decltype(tl_)::v;
      constexpr bool pre = CS0 && tl == 0;
      const f32x4 z = {};
      f32x4 acc[2][6];
      unsigned g0 = 0;
      if (pre) {
#pragma unroll
        for (int i = 0; i < 12; i++) { acc[i & 1][i >> 1] = fold(tl, i & 1, i >> 1, z); asm volatile("" : "+v"(acc[i & 1][i >> 1])); }
        asm volatile("s_nop 7" ::: "memory");
      }
      SB();
      sfor<0, 120>([&](auto i_) __attribute__((always_inline)) {
        {
          constexpr int s2 = decltype(i_)::v / 6, u = decltype(i_)::v % 6, q = 40 * tl + 2 * s2, f = 3 * q + u, ks = 2 * s2 + u / 3;
          if (u % DWE == 0 && f + NB - 1 < 240) __builtin_amdgcn_s_waitcnt(0xc07f | (2 * (NB - 1 - DWE)) << 8);
          if (EDW && tl == 1 && (s2 == 7 || s2 == 14) && u == 0) bar();
          if (HG && tl == 1 && s2 == 1 && u == 0) bar();
          if (u == 0) asm volatile("" ::"v"(a[(q + 1) % WIN]));
          if (DTC && tl == 0 && ks == DTC - 1 && u % 3 == 0) tch = bl1<DEV>(rws, tca, hb);
          if (DTC && tl == 0 && s2 == 16 && u == 0) asm volatile("" ::"v"(tch));
          mm16i(acc[0][u], F8(wb[f % NB]), a[q % WIN], !s2 && !pre);
          SB();
          if (f + NB - 1 < 240) rdh(f + NB - 1, 0);
          u32x2 cf;
          if (!pre && s2 == (tl ? DF1 : DF0)) { cf = cfr((u + 3) % 6); asm volatile("" : "+v"(cf)); }
          if constexpr (u == 1 && q >= 2 && q - 2 + WIN < 80) a[(q - 2) % WIN] = lda(q - 2 + WIN);
          if constexpr (u == 4 && q >= 2 && q - 1 + WIN < 80) a[(q - 1) % WIN] = lda(q - 1 + WIN);
          SB();
          mm16i(acc[1][u], F8(wb[f % NB]), a[(q + 1) % WIN], !s2 && !pre);
          SB();
          if (f + NB - 1 < 240) rdh(f + NB - 1, 1);
          if (tl == 1) {
            if (ks == EG0 && u % 3 == 0) g0 = bl1<DEV>(rws, egate(G, tk), eb);
            if (ks == EG1 && u % 3 == 0 && RARE(anyl(g0 == ~0u))) gate1(rws, egate(G, tk), eb, budget);
            if constexpr (ks >= EG1) {
              constexpr int i = (ks - EG1) * EN / (40 - EG1) + u % 3;
              if constexpr (i < (ks + 1 - EG1) * EN / (40 - EG1)) ea[i & 1][i >> 1] = elda(l, 0, i & 1, i >> 1);
            }
          }
          if (!pre && s2 == (tl ? DF1 : DF0)) {
            SB();
            asm volatile("v_mfma_f32_4x4x4_16b_bf16 %0, %2, %3, %0\n\tv_mfma_f32_4x4x4_16b_bf16 %1, %2, %4, %1"
                         : "+v"(acc[0][(u + 3) % 6]), "+v"(acc[1][(u + 3) % 6]) : "v"(cf), "v"(mf[2 * tl]), "v"(mf[2 * tl + 1]));
          }
          if (!CS0 && tl == 0 && u % 3 == 1 && ks == Y0Q) {
            bar();
#pragma unroll
            for (int i = 0; i < 6; i++) {
              const unsigned o = (l >> 4) * 64 + (l & 3) * 16, o2 = ((i >> 1) + 3 * (i & 1)) * 256;
              cs[i] = CSL2 ? lds16(sm + (L_CS + o) + o2) : bl4<DEV>(rws, o + o2, OFF_C2 + b * 2048);
            }
          }
          if (!CS0 && CSL2 && tl == 0 && s2 == DF0 && u == 0) bar();
          if constexpr (!CS0 && tl == 0 && s2 == DF0 - 1) cvt(u);
          SB();
        }
      });
      asm volatile("s_nop 7" ::: "memory");
      SB();
      auto rdw0 = [&]() __attribute__((always_inline)) {
        const unsigned wr = L_WE + wv * 10240 + (8 * G + (tk >> 2)) * 160 + (l & 3) * 8;
        lds_t e0 = sm + (wr + (G & 1) * 640), e1 = sm + (wr + ((G & 1) ^ 1) * 640);
#pragma unroll
        for (int i = 0; i < 10; i++) { wb0[i].a = trd(e0 + (i / 5) * 5120 + (i % 5) * 32); wb0[i].b = trd(e1 + (i / 5) * 5120 + (i % 5) * 32); }
        SB();
      };
      if (EDW && tl == 1) rdw0();
      if (RARE(anyl(isn(acc[0][0][0] + acc[1][0][0])) || (FRD == tl + 1 && (b & 1)))) {
        RCNT(20 + tl);
        constexpr bool rl = EDW && tl == 1;
        const rsrc_t rg = mkr(wg, 640 * 3072), ru = mkr(wu, 640 * 3072);
        const unsigned cn = L_WE + wv * 10240;
#pragma unroll
        for (int i = 0; i < 12; i++) acc[i & 1][i >> 1] = fold(tl, i & 1, i >> 1, z);
#pragma unroll 1
        for (int s2 = 0; s2 < 20; s2++) {
          u32x4 c0 = fetch16(rws, l * 16, hb + tl * 163840 + s2 * 1024, budget), c1 = fetch16(rws, l * 16, hb + tl * 163840 + 20480 + s2 * 1024, budget);
          if (rl) {
            fence();
#pragma unroll 1
            for (int i = 0; i < 3; i++) {
              const unsigned vo = gu_off(l, i, s2 >= 16), so = s2 < 16 ? gu_blk(s2) : (s2 - 16) * 160 * 3072 + fs * 96;
              sts16(sm + (cn + i * 1024 + l * 16), bl4<0>(rg, vo, so));
              sts16(sm + (cn + 3072 + i * 1024 + l * 16), bl4<0>(ru, vo, so));
            }
            fence();
          }
#pragma unroll
          for (int u = 0; u < 6; u++) {
            const int o = (rl ? cn + (u & 1) * 3072 : s2 * 3072 + (u & 1) * L_UP) + (u >> 1) * 32;
            const bf16x8 w = trd2(p0 + o, p1 + o);
            acc[0][u] = mm16(w, B8(c0), acc[0][u]);
            acc[1][u] = mm16(w, B8(c1), acc[1][u]);
          }
        }
      }
      if (tl == 1) {
        if (!EDW) rdw0();
        bar_lgkm();
      }
      TSM(14 + tl);
      if (HG && tl == 0) {
        lds_t hs = sm + (L_WE + wv * 10240 + l * 16);
#pragma unroll
        for (int i = 0; i < 6; i++) {
          const f32x4 ga = acc[i < 4 ? i >> 1 : i & 1][i < 4 ? 2 * (i & 1) : 4], ua = acc[i < 4 ? i >> 1 : i & 1][i < 4 ? 2 * (i & 1) + 1 : 5];
          sts16(hs + i * 1024, u32x4{pk(ga[0], ga[1]), pk(ga[2], ga[3]), pk(ua[0], ua[1]), pk(ua[2], ua[3])});
        }
        *(__attribute__((address_space(3))) f32x2*)(hs + (6144 - l * 8)) = f32x2{rs[0], rs[1]};
        SB();
      } else {
        const GelK gk[2] = {gelk(rs[2 * tl]), gelk(rs[2 * tl + 1])};
        act_out<false>([&](int i) {
          const int tt = i < 4 ? i >> 1 : i & 1, t = i < 4 ? i & 1 : 2;
          const f32x4 ga = acc[tt][2 * t], ua = acc[tt][2 * t + 1];
          return u32x2{gel2(f32x2{ga[0], ga[1]}, f32x2{ua[0], ua[1]}, gk[tt]), gel2(f32x2{ga[2], ga[3]}, f32x2{ua[2], ua[3]}, gk[tt])};
        }, rws, l, OFF_ACT + (TA + 4 * tl) * 98304 + (fs >> 1) * 3072, fs);
      }
      if (tl == 1) {
        SB();
#pragma unroll
        for (int i = EN; i < 24; i++) ea[i & 1][i >> 1] = elda(l, 0, i & 1, i >> 1);
        SB();
      }
    };
    tile(IC<0>{});
    tile(IC<1>{});
  }
  {
    TSM(16);
    LANES() WFR()
    const unsigned mb = EDW ? (wv >> 1) * L_UP + (wv & 1) * 10240 : wv * 30720;
    lds_t e0 = sm + (L_WE + wv * 10240 + w0), e1 = sm + (L_WE + wv * 10240 + w1), m0 = sm + (mb + w0), m1 = sm + (mb + w1);
    lds_t rs0 = sm + (mb + (EDW ? 20480 : 10240) + l * 16);
    f32x4 acc[2][2][5];
    constexpr int NB = NBE;
    Fr wb[20];
    auto rdh = [&](int q, int s) {
      const int p = q / 5, o = (p < 2 ? p : p < 8 ? p - 2 : p < 10 ? p - 8 : p - 10) * 5120 + (q % 5) * 32;
      if (p == 2 || p == 3) { if (s) wb[q % 20].b = wb0[q - 10].b; else wb[q % 20].a = wb0[q - 10].a; }
      else if (s) wb[q % 20].b = trd((p == 8 || p == 9 ? e1 : m1) + o);
      else wb[q % 20].a = trd((p == 8 || p == 9 ? e0 : m0) + o);
    };
    auto redo = [&](f32x4 (&ac)[2][5], unsigned ab) __attribute__((always_inline)) {
      const rsrc_t rd = mkr(wd, 1536 * 1280);
#pragma unroll
      for (int t = 0; t < 5; t++) ac[0][t] = ac[1][t] = f32x4{};
#pragma unroll 1
      for (int i = 0; i < 12; i++) {
        u32x4 c0 = fetch16(rws, l * 16, ab + i * 1024, budget), c1 = fetch16(rws, l * 16, ab + 49152 + i * 1024, budget);
#pragma unroll
        for (int t = 0; t < 5; t++) {
          u32x4 f;
#pragma unroll
          for (int e = 0; e < 4; e++) {
            const int rw = i % 3 == 2 ? 48 * (G >> 1) + 4 * (G & 1) + 8 * (e >> 1) : 4 * G + 16 * (e >> 1);
            const unsigned vo = (rw + 2 * (e & 1)) * 1280 + tk * 2 + t * 32, s2 = wd_blk(wv, i);
            f[e] = (unsigned)__builtin_amdgcn_raw_buffer_load_b16(rd, vo, s2, 0) | ((unsigned)__builtin_amdgcn_raw_buffer_load_b16(rd, vo + 1280, s2, 0) << 16);
          }
          ac[0][t] = mm16(B8(f), B8(c0), ac[0][t]);
          ac[1][t] = mm16(B8(f), B8(c1), ac[1][t]);
        }
      }
    };
    Fr kp[10], wr_[NB + 1];
    auto rdq = [&](int q, int s) __attribute__((always_inline)) {
      const int p = q / 5, sl = p < 8 ? p - 2 : p < 10 ? p - 8 : p - 10, o = (EDW ? (sl >> 1) * 20480 + (sl & 1) * 5120 : sl * 5120) + (q % 5) * 32;
      Fr& d = q < 20 ? kp[q % 10] : wr_[q % (NB + 1)];
      if (s) d.b = trd((p == 8 || p == 9 ? e1 : m1) + o);
      else d.a = trd((p == 8 || p == 9 ? e0 : m0) + o);
    };
    auto half2 = [&](auto hf_) __attribute__((always_inline)) {
      constexpr int hf = decltype(hf_)::v;
      sfor<0, 60>([&](auto u_) __attribute__((always_inline)) {
        constexpr int u = decltype(u_)::v, p = u / 5, t = u % 5, q = hf == 0 && u < 10 ? u + 10 : u + NB;
        const bool more = q < 60 && (q >= 20 || (hf == 0 && u < 10));
        const Fr& w = u < 10 ? wb0[u % 10] : u < 20 ? kp[u % 10] : wr_[u % (NB + 1)];
        if (hf == 0 && u >= 10 && more && q % 5 == 0 && ((EDW ? 0x180 : EBM) >> (q / 5 - 3) & 1)) bar();
        if (EDW && PGC > 8 && hf == 0 && u == UG) bar();
        if (EDW && hf == 0 && u == UZ) asm volatile("s_waitcnt lgkmcnt(%0)\n\ts_barrier" ::"n"(imin(2 * (UZ + NB < 20 ? 0 : UZ + NB - 20), 15)) : "memory");
        constexpr int v = 60 * hf + u - (EDW && PGC > 8 ? UG : 15 + 5 * PGC - NB);
        if constexpr (v >= 0 && v % ELS == 0 && v / ELS < 24) { ea[v / ELS & 1][v / ELS >> 1] = elda(l, 1, v / ELS & 1, v / ELS >> 1); SB(); }
        if constexpr (TRO && hf == 1 && u >= 50) {
          constexpr int u0 = 50 + 2 * (u % 5), tt = u >= 55;
          mm16i(acc[1][tt][u0 % 5], F8(wr_[u0 % (NB + 1)]), ea[tt][u0 / 5], false);
          SB();
          if (tt) stsf4(rs0 + 20480 + (u - 55) * 1024, acc[1][0][u - 55]);
          SB();
          mm16i(acc[1][tt][(u0 + 1) % 5], F8(wr_[(u0 + 1) % (NB + 1)]), ea[tt][(u0 + 1) / 5], false);
          SB();
          return;
        }
        mm16i(acc[hf][0][t], F8(w), ea[0][p], p == 0);
        SB();
        if (more) rdq(q, 0);
        SB();
        mm16i(acc[hf][1][t], F8(w), ea[1][p], p == 0);
        SB();
        if (more) rdq(q, 1);
        if (60 * hf + u == SU0) TSX(20);
        if (60 * hf + u == SU1) TSX(21);
        if constexpr (hf == 1 && u >= RS0 && (u - RS0) % RSS == 0 && (u - RS0) / RSS < 10) stsf4(rs0 + (u - RS0) / RSS * 1024, acc[0][(u - RS0) / RSS / 5][(u - RS0) / RSS % 5]);
        SB();
      });
    };
    if (!EDW) bar();
#pragma unroll
    for (int q = 0; q < (ESC == 2 ? 0 : NB); q++) { rdh(q, 0); rdh(q, 1); }
    SB();
    auto half = [&](auto hf_) __attribute__((always_inline)) {
      constexpr int hf = decltype(hf_)::v;
#pragma unroll
      for (int u = 0; u < 60; u++) {
        const int p = u / 5, t = u % 5;
        const bool more = u + NB < 60 && (hf == 0 || u + NB >= 20);
        if (u + NB == 40 || (hf == 0 && u + NB == 50)) bar();
        if (ESC && ((hf == 0 && (u + NB == 30 || (ZD2 && u + NB == 55))) || (ZF2 && hf == 1 && u + NB == 50))) bar();
        if (ESC && hf == 0 && u == UG) bar();
        if (ESC && hf == 0 && u >= UG && u < UG + EW) { ea[0][u - UG] = elda(l, 1, 0, u - UG); ea[1][u - UG] = elda(l, 1, 1, u - UG); SB(); }
        mm16i(acc[hf][0][t], F8(wb[u % 20]), ea[0][p], p == 0);
        SB();
        if (more) rdh(u + NB, 0);
        SB();
        mm16i(acc[hf][1][t], F8(wb[u % 20]), ea[1][p], p == 0);
        SB();
        if (hf == 0 && ((!ESC && u == 9) || u == 59)) bar();
        if (more) rdh(u + NB, 1);
        if (hf == 1 && t == 4 && p + EW < 12) { ea[0][p + EW] = elda(l, 1, 0, p + EW); ea[1][p + EW] = elda(l, 1, 1, p + EW); }
        if (60 * hf + u == SU0) TSX(20);
        if (60 * hf + u == SU1) TSX(21);
        if (hf == 1 && u >= RS0 && (u - RS0) % RSS == 0 && (u - RS0) / RSS < 10 && EXPV != 33) stsf4(rs0 + (u - RS0) / RSS * 1024, acc[0][(u - RS0) / RSS / 5][(u - RS0) / RSS % 5]);
        SB();
      }
    };
    if constexpr (ESC == 2) half2(IC<0>{}); else half(IC<0>{});
    TSM(17);
    if (!HGATE) gate1(rws, egate(G, tk), eb + 4 * 98304, budget);
    TSX(19);
#pragma unroll
    for (int i = 0; i < (ESC ? 0 : EW); i++) { ea[0][i] = elda(l, 1, 0, i); ea[1][i] = elda(l, 1, 1, i); }
    SB();
    if (EXPV == 34) { __builtin_amdgcn_s_waitcnt(0x0f70); TSX(19); }
    if (RARE(anyl(isn(acc[0][0][0][0] + acc[0][1][0][0])))) redo(acc[0], eb);
    SB();
    if constexpr (ESC == 2) half2(IC<1>{}); else half(IC<1>{});
    if (AMF) { asm volatile("s_nop 7" ::: "memory"); SB(); }
    if (RARE(anyl(isn(acc[1][0][0][0] + acc[1][1][0][0])) || (FRD == 3 && (b & 1)))) {
      redo(acc[1], eb + 4 * 98304);
#pragma unroll
      for (int i = 0; i < (TRO ? 5 : 0); i++) stsf4(rs0 + 20480 + i * 1024, acc[1][0][i]);
    }
    TSM(18);
    if (EXPV == 33) {
#pragma unroll
      for (int i = 0; i < 10; i++) stsf4(rs0 + i * 1024, acc[0][i / 5][i % 5]);
    }
#pragma unroll
    for (int i = TRO ? 5 : 0; i < 10; i++) stsf4(rs0 + (EDW ? 20480 : 10240) + i * 1024, acc[1][i / 5][i % 5]);
    SB();
    constexpr int RO = EDW ? 10240 : 30720;
    lds_t ra_ = sm + ((EDW ? 4 + 4 * wm + kh : 2 + 2 * wm + kh) * 5120 + l * 16), rc_ = ra_ + 61440;
    unsigned oa = tok * 1280 + G * 8;
    f32x4 hf[5], pa[5][4];
#pragma unroll
    for (int t = 0; t < 5; t++) { hf[t] = f32x4{blo(hh[t][0]), bhi(hh[t][0]), blo(hh[t][1]), bhi(hh[t][1])}; asm volatile("" : "+v"(hf[t])); }
    asm volatile("" : "+v"(ra_), "+v"(rc_), "+v"(oa));
    const rsrc_t ro = mkr(out, 2048 * 1280);
    SB();
    bar_lgkm();
    if (SU0 == 200) TSX(20);
#pragma unroll
    for (int t = 0; t < 5; t++) { pa[t][0] = ldsf4(ra_ + t * 1024); pa[t][1] = ldsf4(ra_ + RO + t * 1024); pa[t][2] = ldsf4(rc_ + t * 1024); pa[t][3] = ldsf4(rc_ + RO + t * 1024); SB(); }
#pragma unroll
    for (int t = 0; t < 5; t++) {
      const f32x4 o = ((pa[t][0] + pa[t][1]) + (pa[t][2] + pa[t][3])) + hf[t];
      bs2<OAUX>(u32x2{pk(o[0], o[1]), pk(o[2], o[3])}, ro, oa + t * 32, jc * 160);
      SB();
    }
    if (SU0 == 200) TSX(21);
  }
  TSW(1);
}
extern "C" __global__ void hk_tail() {}

extern "C" void kernel_launch(void* const* d_in, const int* in_sizes, int n_in, void* d_out, int out_size, void* d_ws, size_t ws_size,
                              hipStream_t stream) {
  const u16* x = (const u16*)d_in[0];
  const u16* wqkv = (const u16*)d_in[1];
  const u16* wo = (const u16*)d_in[2];
  const u16* wg = (const u16*)d_in[4];
  const u16* wu = (const u16*)d_in[5];
  const u16* wd = (const u16*)d_in[6];
  char* ws = (char*)d_ws;
  constexpr int LM = 163840;
  (void)hipFuncSetAttribute((const void*)hk_main, hipFuncAttributeMaxDynamicSharedMemorySize, LM);
  hk_main<<<256, 512, LM, stream>>>(x, wqkv, wo, wg, wu, wd, ws, (u16*)d_out);
}
```
